```python
import torch

def _softmax(x, axis=-1):
    return torch.softmax(x, dim=axis)
def _gelu(x):
    return torch.nn.functional.gelu(x, approximate="tanh")
def _silu(x):
    return torch.nn.functional.silu(x)
def _sigmoid(x):
    return torch.sigmoid(x)
def _layernorm(x, axis=-1, eps=1e-5):
    m = x.mean(dim=axis, keepdim=True)
    v = x.var(dim=axis, keepdim=True, unbiased=False)
    return (x - m) / torch.sqrt(v + eps)
def _scatter_add(tgt, idx, src, axis=0):
    return tgt.clone().index_add_(axis, idx, src)
def _segment_sum(src, seg, n):
    out = torch.zeros((n,) + src.shape[1:], dtype=src.dtype)
    return out.index_add_(0, seg, src)
def _pad(x, pw):
    flat = tuple(v for pair in reversed(pw) for v in pair)
    return torch.nn.functional.pad(x, flat)

kernel_name = 'sequential_preLN__ssm_conv_s4__glu_gated_bilinear__prompt'

def setup_inputs():

    _w = (lambda a: (lambda t: t.to(torch.bfloat16) if t.is_floating_point() else t)(torch.as_tensor(a)))
    def _A_setup():
        B, L, D, N = 1, 2048, 512, 128
        A_re = -(lambda _x: torch.exp(torch.as_tensor(_x)))((lambda *s: torch.randn(*s, dtype=torch.bfloat16) * 6.4)(D, N))
        A_im = torch.pi * (lambda *s: torch.randn(*s, dtype=torch.bfloat16) * 0.05)(D, N)
        B_re = (lambda *s: torch.randn(*s, dtype=torch.bfloat16) * 0.1)(D, N)
        B_im = (lambda *s: torch.randn(*s, dtype=torch.bfloat16) * 0.1)(D, N)
        C_re = (lambda *s: torch.randn(*s, dtype=torch.bfloat16) * 0.1)(D, N)
        C_im = (lambda *s: torch.randn(*s, dtype=torch.bfloat16) * 0.1)(D, N)
        log_dt = (lambda *s: torch.randn(*s, dtype=torch.bfloat16) * 0.05)(D) * 0.5 - 4.0
        Dskip = (lambda *s: torch.randn(*s, dtype=torch.bfloat16) * 0.05)(D)
        return {
            "u": _w((lambda *s: torch.randn(*s, dtype=torch.bfloat16) * 0.05)(B, L, D)),
            "A_re": _w(A_re),
            "A_im": _w(A_im),
            "B_re": _w(B_re),
            "B_im": _w(B_im),
            "C_re": _w(C_re),
            "C_im": _w(C_im),
            "log_dt": _w(log_dt),
            "Dskip": _w(Dskip),
        }
    _A_in = _A_setup()

    def _B_setup():
        B, S, D, F = 1, 2048, 512, 1792
        return {
            "x": _w((lambda *s: torch.randn(*s, dtype=torch.bfloat16) * 0.05)(B, S, D)),
            "w_gate": _w((lambda *s: torch.randn(*s, dtype=torch.bfloat16) * 0.4)(D, F) * (D ** -0.5)),
            "w_up": _w((lambda *s: torch.randn(*s, dtype=torch.bfloat16) * 0.4)(D, F) * (D ** -0.5)),
            "w_down": _w((lambda *s: torch.randn(*s, dtype=torch.bfloat16) * 0.4)(F, D) * (F ** -0.5)),
        }
    _B_in = _B_setup()

    return {
        "A_u": _A_in["u"],
        "A_A_re": _A_in["A_re"],
        "A_A_im": _A_in["A_im"],
        "A_B_re": _A_in["B_re"],
        "A_B_im": _A_in["B_im"],
        "A_C_re": _A_in["C_re"],
        "A_C_im": _A_in["C_im"],
        "A_log_dt": _A_in["log_dt"],
        "A_Dskip": _A_in["Dskip"],
        "B_x": _B_in["x"],
        "B_w_gate": _B_in["w_gate"],
        "B_w_up": _B_in["w_up"],
        "B_w_down": _B_in["w_down"],
    }

def reference(A_u, A_A_re, A_A_im, A_B_re, A_B_im, A_C_re, A_C_im, A_log_dt, A_Dskip, B_x, B_w_gate, B_w_up, B_w_down):
    def _A_ref(u, A_re, A_im, B_re, B_im, C_re, C_im, log_dt, Dskip):
        Bsz, L, D = u.shape
        dt = torch.exp(log_dt)[:, None]
        A = A_re + 1j * A_im
        Bc = B_re + 1j * B_im
        Cc = C_re + 1j * C_im
        logdA = dt * A
        dB = dt * Bc
        l = (lambda x, dt: x.to(dt))(torch.arange(L), A_re.dtype)
        P = torch.exp(logdA[:, :, None] * l[None, None, :])
        K = torch.einsum("dn,dnl,dn->dl", Cc, P, dB).real
        L2 = 2 * L
        Kp = _pad(K, ((0, 0), (0, L)))
        up = (lambda x, *d: x.permute(*d))(u, 0, 2, 1)
        up = _pad(up, ((0, 0), (0, 0), (0, L)))
        Kf = (lambda x, axes=(-2,-1): torch.fft.rfft2(x.float(), dim=axes))(Kp[:, None, :], axes=(-2, -1))
        Uf = (lambda x, axes=(-2,-1): torch.fft.rfft2(x.float(), dim=axes))(up[:, :, None, :], axes=(-2, -1))
        Yf = Uf * Kf[None, :, :, :]
        y = (lambda x, s, axes=(-2,-1): torch.fft.irfft2(x, s=s, dim=axes).to(torch.bfloat16))(Yf, (1, L2), axes=(-2, -1))[:, :, 0, :L]
        y = (lambda x, *d: x.permute(*d))(y, 0, 2, 1) + u * Dskip[None, None, :]
        return y

    def _B_ref(x, w_gate, w_up, w_down):
        g = torch.einsum("bsd,df->bsf", x, w_gate)
        u = torch.einsum("bsd,df->bsf", x, w_up)
        h = g * u
        return torch.einsum("bsf,fd->bsd", h, w_down)

    x = A_u
    h = x + _A_ref(_layernorm(x, axis=-1), A_A_re, A_A_im, A_B_re, A_B_im, A_C_re, A_C_im, A_log_dt, A_Dskip)
    h2 = h + _B_ref(_layernorm(h, axis=-1), B_w_gate, B_w_up, B_w_down)
    return h2
```

```cpp
#include <hip/hip_runtime.h>

__global__ void __launch_bounds__(256, 1)
s4_glu_block(const void* x, const void* a_re, const void* a_im, const void* b_re, const void* b_im, const void* c_re,
             const void* c_im, const void* log_dt, const void* dskip, const void* w_gate, const void* w_up,
             const void* w_down, void* out, void* ws) {
  __shared__ __attribute__((aligned(16))) unsigned char lds[163840];
  unsigned tid = threadIdx.x;
  asm volatile("" ::"s"((unsigned)(size_t)(__attribute__((address_space(3))) unsigned char*)lds));
  asm volatile(
    ".Lmk_start:\n"
    "s_load_dwordx16 s[4:19], %1, 0\n"
    "s_load_dwordx8 s[20:27], %1, 64\n"
    "s_load_dwordx4 s[28:31], %1, 96\n"
    "s_lshr_b32 s58, %2, 3\n"
    "s_getpc_b64 s[86:87]\n"
    ".Lmk_pc:\n"
    "v_lshrrev_b32 v17, 5, v0\n"
    "v_lshl_add_u32 v17, s58, 3, v17\n"
    "v_lshlrev_b32 v17, 7, v17\n"
    "v_min_u32 v17, .Lmk_end-.Lmk_pc-4, v17\n"
    "global_load_dword v16, v17, s[86:87]\n"
    "v_lshrrev_b32 v2, 6, v0\n"
    "s_and_b32 s57, %2, 7\n"
    "v_readfirstlane_b32 s56, v2\n"
    "v_and_b32 v1, 63, v0\n"
    "s_lshl_b32 s83, s57, 5\n"
    "s_add_u32 s83, s83, s58\n"
    "v_and_b32 v3, 3, v1\n"
    "v_lshrrev_b32 v15, 2, v1\n"
    "s_lshl_b32 s67, s83, 13\n"
    "s_lshl_b32 s68, s56, 8\n"
    "s_add_u32 s67, s67, s68\n"
    "v_lshl_add_u32 v11, v3, 11, s67\n"
    "v_lshl_add_u32 v11, v15, 4, v11\n"
    "s_waitcnt lgkmcnt(0)\n"
    "global_load_dwordx4 v[248:251], v11, s[4:5]\n"
    "global_load_dwordx4 v[252:255], v11, s[4:5] offset:1024\n"
    "s_mov_b32 s3, %2\n"
    "s_lshr_b32 s59, s56, 1\n"
    "s_and_b32 s60, s56, 1\n"
    "v_lshlrev_b32 v8, 3, v3\n"
    "s_lshl1_add_u32 s66, s83, s59\n"
    "s_lshl_b32 s67, s66, 8\n"
    "v_lshl_add_u32 v12, v1, 2, s67\n"
    "v_and_b32 v6, 7, v1\n"
    "v_lshrrev_b32 v10, 3, v0\n"
    "v_mul_u32_u24 v13, 3584, v10\n"
    "v_lshl_add_u32 v13, v6, 4, v13\n"
    "s_mul_i32 s80, s57, 229376\n"
    "s_lshl_b32 s67, s58, 7\n"
    "s_add_u32 s80, s80, s67\n"
    "s_add_u32 s81, s80, 114688\n"
    "s_lshl_b32 s69, s83, 2\n"
    "s_cmp_lt_u32 s3, 224\n"
    "s_cselect_b32 s67, 1835008, 0\n"
    "s_cselect_b32 s68, 26214400, 0\n"
    "s_load_dword s71, s[30:31], 0\n"
    "s_load_dword s74, s[18:19], s69\n"
    "s_load_dword s75, s[20:21], s69\n"
    "s_mov_b32 s40, s22\n"
    "s_and_b32 s41, s23, 0xffff\n"
    "s_mov_b32 s42, s67\n"
    "s_mov_b32 s43, 0x20000\n"
    "s_mov_b32 s44, s24\n"
    "s_and_b32 s45, s25, 0xffff\n"
    "s_mov_b32 s46, s67\n"
    "s_mov_b32 s47, 0x20000\n"
    "global_load_dword v18, v12, s[6:7]\n"
    "global_load_dword v19, v12, s[8:9]\n"
    "global_load_dword v20, v12, s[10:11]\n"
    "global_load_dword v21, v12, s[12:13]\n"
    "global_load_dword v22, v12, s[14:15]\n"
    "global_load_dword v23, v12, s[16:17]\n"
    "s_mov_b32 s48, s26\n"
    "s_and_b32 s49, s27, 0xffff\n"
    "s_mov_b32 s50, s67\n"
    "s_mov_b32 s51, 0x20000\n"
    "v_lshlrev_b32 v14, 10, v10\n"
    "v_lshl_add_u32 v14, v6, 4, v14\n"
    "s_mul_i32 s84, s57, 28\n"
    "s_add_u32 s84, s84, s58\n"
    "s_lshr_b32 s67, s84, 2\n"
    "s_lshl_b32 s82, s67, 15\n"
    "s_and_b32 s67, s84, 3\n"
    "s_lshl_b32 s67, s67, 8\n"
    "s_add_u32 s82, s82, s67\n"
    "buffer_load_dwordx4 v[232:235], v13, s[40:43], s80 offen\n"
    "buffer_load_dwordx4 v[236:239], v13, s[44:47], s80 offen\n"
    "buffer_load_dwordx4 v[240:243], v13, s[40:43], s81 offen\n"
    "buffer_load_dwordx4 v[244:247], v13, s[44:47], s81 offen\n"
    "buffer_load_dwordx4 v[224:227], v14, s[48:51], s82 offen\n"
    "buffer_load_dwordx4 v[228:231], v14, s[48:51], s82 offen offset:128\n"
    "s_mov_b32 s32, s30\n"
    "s_and_b32 s33, s31, 0xffff\n"
    "s_mov_b32 s34, 26214400\n"
    "s_mov_b32 s35, 0x20000\n"
    "s_mov_b32 s36, s30\n"
    "s_and_b32 s37, s31, 0xffff\n"
    "s_mov_b32 s38, s68\n"
    "s_mov_b32 s39, 0x20000\n"
    "s_mov_b32 s52, s28\n"
    "s_and_b32 s53, s29, 0xffff\n"
    "s_mov_b32 s54, 2097152\n"
    "s_mov_b32 s55, 0x20000\n"
    "s_mov_b32 s79, 0x3f803f80\n"
    "v_and_b32 v11, 15, v1\n"
    "v_lshrrev_b32 v12, 4, v1\n"
    "v_lshlrev_b32 v24, 4, v1\n"
    "v_lshrrev_b32 v7, 3, v1\n"
    "v_and_b32 v9, 3, v15\n"
    "v_lshlrev_b32 v28, 1, v8\n"
    "v_lshlrev_b32 v26, 2, v8\n"
    "v_add_u32 v26, 161792, v26\n"
    "v_and_b32 v2, 1, v12\n"
    "v_lshl_add_u32 v25, v2, 4, v26\n"
    "v_lshrrev_b32 v2, 5, v1\n"
    "v_lshl_add_u32 v25, v2, 7, v25\n"
    "s_lshl_b32 s67, s56, 2\n"
    "v_add_u32 v25, s67, v25\n"
    "s_lshl_b32 s67, s56, 10\n"
    "v_add_u32 v27, s67, v24\n"
    "v_add_u32 v5, 4096, v27\n"
    "s_lshl_b32 s4, s83, 14\n"
    "s_add_u32 s4, s4, 2097152\n"
    "s_add_u32 s5, s4, 8192\n"
    "s_lshl_b32 s6, s83, 6\n"
    "s_add_u32 s6, s6, 81920\n"
    "s_lshl_b64 s[8:9], 1, s56\n"
    "s_cmp_eq_u32 s56, 0\n"
    "s_cselect_b32 s6, s6, 26214400\n"
    "s_mov_b64 s[8:9], 15\n"
    "v_mov_b32 v29, v5\n"
    "v_mul_u32_u24 v3, 288, v10\n"
    "v_add_u32 v3, 131072, v3\n"
    "v_lshl_add_u32 v39, v6, 5, v3\n"
    "v_lshl_add_u32 v40, v6, 4, v3\n"
    "s_lshl_b32 s67, s56, 6\n"
    "s_add_u32 s67, s67, 131072\n"
    "v_add_u32 v4, s67, v8\n"
    "v_lshl_add_u32 v43, v12, 2, v9\n"
    "v_mul_u32_u24 v41, 288, v43\n"
    "v_add_u32 v41, v41, v4\n"
    "v_lshl_add_u32 v3, v12, 3, v9\n"
    "v_mul_u32_u24 v42, 288, v3\n"
    "v_add_u32 v42, v42, v4\n"
    "s_lshl_b32 s68, s56, 1\n"
    "s_lshl3_add_u32 s67, s58, s68\n"
    "s_lshl_b32 s69, s57, 1\n"
    "s_lshl4_add_u32 s67, s67, s69\n"
    "s_lshl_b32 s7, s67, 10\n"
    "s_add_u32 s7, s7, 10485760\n"
    "s_add_u32 s10, s7, 16384\n"
    "s_lshl_b32 s12, s67, 5\n"
    "s_add_u32 s12, s12, 393216\n"
    "s_lshl3_add_u32 s11, s84, s68\n"
    "s_lshl_b32 s11, s11, 10\n"
    "s_add_u32 s11, s11, 14155776\n"
    "s_lshl_b32 s67, s56, 14\n"
    "v_and_b32 v2, 1, v1\n"
    "v_lshl_add_u32 v44, v2, 13, s67\n"
    "v_lshrrev_b32 v2, 1, v1\n"
    "v_lshl_add_u32 v44, v2, 4, v44\n"
    "v_lshlrev_b32 v45, 5, v0\n"
    "v_lshlrev_b32 v47, 4, v0\n"
    "s_lshl_b32 s13, s3, 5\n"
    "s_lshl_b32 s14, s57, 13\n"
    "s_lshl_b32 s67, s56, 20\n"
    "v_lshl_add_u32 v46, v15, 14, s67\n"
    "v_lshl_add_u32 v46, v8, 1, v46\n"
    "s_and_b32 s67, s83, 3\n"
    "s_lshl_b32 s67, s67, 12\n"
    "s_lshr_b32 s68, s83, 2\n"
    "s_lshl_b32 s68, s68, 6\n"
    "s_add_u32 s67, s67, s68\n"
    "s_add_u32 s16, s67, 2097152\n"
    "s_add_u32 s17, s67, 2359296\n"
    "s_add_u32 s18, s67, 2621440\n"
    "s_add_u32 s19, s67, 2883584\n"
    "s_mov_b32 s20, 81920\n"
    "s_mov_b32 s21, 86016\n"
    "s_mov_b32 s22, 90112\n"
    "s_mov_b32 s23, 94208\n"
    "v_add_u32 v56, 65536, v47\n"
    "s_mov_b32 s24, 0x05040100\n"
    "s_mov_b32 s25, 0x07060302\n"
    "s_mov_b32 s26, 0x3b000000\n"
    "s_mov_b32 s28, 0x3727c5ac\n"
    "v_mov_b32 v58, s79\n"
    "v_mov_b32 v59, s79\n"
    "v_mov_b32 v60, s79\n"
    "v_mov_b32 v61, s79\n"
    "s_waitcnt lgkmcnt(0)\n"
    "s_add_u32 s72, s71, 0x9e3779b9\n"
    "s_mov_b32 s73, s72\n"
    "v_mov_b32 v62, s72\n"
    "v_mov_b32 v63, s72\n"
    "v_mov_b32 v65, s72\n"
    "v_mov_b32 v67, s72\n"
    "v_mov_b32 v185, s72\n"
    "v_mov_b32 v187, s72\n"
    "v_mov_b32 v189, s72\n"
    "v_mov_b32 v191, s72\n"
    "v_mov_b32 v193, s72\n"
    "v_mov_b32 v195, s72\n"
    "v_mov_b32 v197, s72\n"
    "v_mov_b32 v199, s72\n"
    "s_cmp_eq_u32 s60, 0\n"
    "s_mov_b32 s67, 0x01000c0c\n"
    "s_mov_b32 s68, 0x03020c0c\n"
    "s_cselect_b32 s30, s67, s68\n"
    "s_cselect_b32 s31, s68, s67\n"
    "s_lshl_b32 s67, s59, 4\n"
    "s_lshr_b32 s74, s74, s67\n"
    "s_lshl_b32 s74, s74, 16\n"
    "s_lshr_b32 s75, s75, s67\n"
    "s_lshl_b32 s75, s75, 16\n"
    "s_mul_i32 s67, s59, 30096\n"
    "s_lshl_b32 s68, s60, 8\n"
    "s_add_u32 m0, s67, s68\n"
    "v_mov_b32 v3, 0x3fb8aa3b\n"
    "v_mul_f32 v2, s74, v3\n"
    "v_fma_f32 v3, s74, v3, -v2\n"
    "v_exp_f32 v2, v2\n"
    "v_mov_b32 v4, 0x32a57060\n"
    "v_fma_f32 v3, s74, v4, v3\n"
    "v_mul_f32 v3, 0x3f317218, v3\n"
    "v_fma_f32 v2, v2, v3, v2\n"
    "v_cvt_pk_bf16_f32 v2, v2, v2\n"
    "v_lshlrev_b32 v68, 16, v2\n"
    "s_waitcnt vmcnt(13)\n"
    "v_dot2_f32_bf16 v78, v248, s79, 0\n"
    "v_dot2_f32_bf16 v79, v248, v248, 0\n"
    "s_waitcnt vmcnt(12)\n"
    "v_dot2_f32_bf16 v80, v252, s79, 0\n"
    "v_dot2_f32_bf16 v81, v252, v252, 0\n"
    "v_dot2_f32_bf16 v78, v249, s79, v78\n"
    "v_dot2_f32_bf16 v79, v249, v249, v79\n"
    "v_dot2_f32_bf16 v80, v253, s79, v80\n"
    "v_dot2_f32_bf16 v81, v253, v253, v81\n"
    "v_dot2_f32_bf16 v78, v250, s79, v78\n"
    "v_dot2_f32_bf16 v79, v250, v250, v79\n"
    "v_dot2_f32_bf16 v80, v254, s79, v80\n"
    "v_dot2_f32_bf16 v81, v254, v254, v81\n"
    "v_dot2_f32_bf16 v78, v251, s79, v78\n"
    "v_dot2_f32_bf16 v79, v251, v251, v79\n"
    "v_dot2_f32_bf16 v80, v255, s79, v80\n"
    "v_dot2_f32_bf16 v81, v255, v255, v81\n"
    "v_lshlrev_b32 v160, 16, v248\n"
    "v_and_b32 v161, 0xffff0000, v248\n"
    "v_lshlrev_b32 v162, 16, v249\n"
    "v_permlane32_swap_b32 v78, v79\n"
    "v_permlane32_swap_b32 v80, v81\n"
    "v_add_f32 v78, v78, v79\n"
    "v_add_f32 v80, v80, v81\n"
    "v_and_b32 v163, 0xffff0000, v249\n"
    "v_lshlrev_b32 v164, 16, v250\n"
    "v_permlane16_swap_b32 v78, v80\n"
    "v_add_f32 v78, v78, v80\n"
    "v_and_b32 v165, 0xffff0000, v250\n"
    "v_lshlrev_b32 v166, 16, v251\n"
    "v_add_f32_dpp v78, v78, v78 row_ror:4 row_mask:0xf bank_mask:0xf\n"
    "v_and_b32 v167, 0xffff0000, v251\n"
    "v_lshlrev_b32 v168, 16, v252\n"
    "v_add_f32_dpp v78, v78, v78 row_ror:8 row_mask:0xf bank_mask:0xf\n"
    "ds_write_b32 v25, v78\n"
    "v_and_b32 v169, 0xffff0000, v252\n"
    "v_lshlrev_b32 v170, 16, v253\n"
    "v_and_b32 v171, 0xffff0000, v253\n"
    "v_lshlrev_b32 v172, 16, v254\n"
    "v_and_b32 v173, 0xffff0000, v254\n"
    "v_lshlrev_b32 v174, 16, v255\n"
    "v_and_b32 v175, 0xffff0000, v255\n"
    "s_waitcnt lgkmcnt(0)\n"
    "s_barrier\n"
    "ds_read_b128 v[144:147], v26 offset:0\n"
    "ds_read_b128 v[148:151], v26 offset:16\n"
    "ds_read_b128 v[152:155], v26 offset:128\n"
    "ds_read_b128 v[156:159], v26 offset:144\n"
    "s_waitcnt lgkmcnt(3)\n"
    "v_pk_add_f32 v[144:145], v[144:145], v[146:147]\n"
    "s_waitcnt lgkmcnt(2)\n"
    "v_pk_add_f32 v[148:149], v[148:149], v[150:151]\n"
    "s_waitcnt lgkmcnt(1)\n"
    "v_pk_add_f32 v[152:153], v[152:153], v[154:155]\n"
    "s_waitcnt lgkmcnt(0)\n"
    "v_pk_add_f32 v[156:157], v[156:157], v[158:159]\n"
    "v_add_f32 v82, v144, v145\n"
    "v_add_f32 v83, v148, v149\n"
    "v_add_f32 v84, v152, v153\n"
    "v_add_f32 v85, v156, v157\n"
    "v_pk_mul_f32 v[82:83], v[82:83], s[26:27] op_sel_hi:[1,0]\n"
    "v_pk_mul_f32 v[84:85], v[84:85], s[26:27] op_sel_hi:[1,0]\n"
    "v_pk_fma_f32 v[84:85], v[82:83], v[82:83], v[84:85] neg_lo:[1,0,0] neg_hi:[1,0,0]\n"
    "v_max_f32 v84, 0, v84\n"
    "v_max_f32 v85, 0, v85\n"
    "v_cvt_pk_bf16_f32 v3, v84, v85\n"
    "v_lshlrev_b32 v84, 16, v3\n"
    "v_and_b32 v85, 0xffff0000, v3\n"
    "v_pk_add_f32 v[84:85], v[84:85], s[28:29] op_sel_hi:[1,0]\n"
    "v_cvt_pk_bf16_f32 v3, v84, v85\n"
    "v_lshlrev_b32 v84, 16, v3\n"
    "v_and_b32 v85, 0xffff0000, v3\n"
    "v_sqrt_f32 v84, v84\n"
    "v_sqrt_f32 v85, v85\n"
    "v_cvt_pk_bf16_f32 v2, v82, v83\n"
    "v_cvt_pk_bf16_f32 v3, v84, v85\n"
    "v_lshlrev_b32 v84, 16, v3\n"
    "v_and_b32 v85, 0xffff0000, v3\n"
    "v_rcp_f32 v86, v84\n"
    "v_rcp_f32 v87, v85\n"
    "v_lshlrev_b32 v82, 16, v2\n"
    "v_and_b32 v83, 0xffff0000, v2\n"
    "v_pk_mul_f32 v[88:89], v[82:83], v[86:87] neg_lo:[1,0] neg_hi:[1,0]\n"
    "v_pk_fma_f32 v[160:161], v[160:161], v[86:87], v[88:89] op_sel:[0,0,0] op_sel_hi:[1,0,0]\n"
    "v_pk_fma_f32 v[168:169], v[168:169], v[86:87], v[88:89] op_sel:[0,1,1] op_sel_hi:[1,1,1]\n"
    "v_cvt_pk_f16_f32 v184, v160, v161\n"
    "v_cvt_pk_f16_f32 v186, v168, v169\n"
    "v_pk_fma_f32 v[162:163], v[162:163], v[86:87], v[88:89] op_sel:[0,0,0] op_sel_hi:[1,0,0]\n"
    "v_pk_fma_f32 v[170:171], v[170:171], v[86:87], v[88:89] op_sel:[0,1,1] op_sel_hi:[1,1,1]\n"
    "v_cvt_pk_f16_f32 v188, v162, v163\n"
    "v_cvt_pk_f16_f32 v190, v170, v171\n"
    "v_pk_fma_f32 v[164:165], v[164:165], v[86:87], v[88:89] op_sel:[0,0,0] op_sel_hi:[1,0,0]\n"
    "v_pk_fma_f32 v[172:173], v[172:173], v[86:87], v[88:89] op_sel:[0,1,1] op_sel_hi:[1,1,1]\n"
    "v_cvt_pk_f16_f32 v192, v164, v165\n"
    "v_cvt_pk_f16_f32 v194, v172, v173\n"
    "v_pk_fma_f32 v[166:167], v[166:167], v[86:87], v[88:89] op_sel:[0,0,0] op_sel_hi:[1,0,0]\n"
    "v_pk_fma_f32 v[174:175], v[174:175], v[86:87], v[88:89] op_sel:[0,1,1] op_sel_hi:[1,1,1]\n"
    "v_cvt_pk_f16_f32 v196, v166, v167\n"
    "v_cvt_pk_f16_f32 v198, v174, v175\n"
    "v_perm_b32 v64, v3, v2, s24\n"
    "v_perm_b32 v66, v3, v2, s25\n"
    "s_waitcnt vmcnt(11)\n"
    "v_perm_b32 v70, 0, v18, s30\n"
    "s_waitcnt vmcnt(10)\n"
    "v_perm_b32 v71, 0, v19, s30\n"
    "s_waitcnt vmcnt(9)\n"
    "v_perm_b32 v176, 0, v20, s30\n"
    "v_perm_b32 v177, 0, v20, s31\n"
    "s_waitcnt vmcnt(8)\n"
    "v_perm_b32 v178, 0, v21, s30\n"
    "v_perm_b32 v179, 0, v21, s31\n"
    "s_waitcnt vmcnt(7)\n"
    "v_perm_b32 v180, 0, v22, s30\n"
    "v_perm_b32 v181, 0, v22, s31\n"
    "s_waitcnt vmcnt(6)\n"
    "v_perm_b32 v182, 0, v23, s30\n"
    "v_perm_b32 v183, 0, v23, s31\n"
    "v_mul_f32 v70, v68, v70\n"
    "v_mul_f32 v71, v68, v71\n"
    "v_mul_f32 v70, 0x3fb8aa3b, v70\n"
    "v_mul_f32 v71, 0x3e22f983, v71\n"
    "v_pk_mul_f32 v[72:73], v[180:181], v[176:177]\n"
    "v_pk_mul_f32 v[74:75], v[180:181], v[178:179]\n"
    "v_pk_fma_f32 v[72:73], v[182:183], v[178:179], v[72:73] neg_lo:[1,0,0] neg_hi:[1,0,0]\n"
    "v_pk_fma_f32 v[74:75], v[182:183], v[176:177], v[74:75]\n"
    "v_max3_f32 v76, |v72|, |v73|, 0\n"
    "v_max3_f32 v76, |v74|, |v75|, v76\n"
    "v_exp_f32 v13, v70\n"
    "v_fract_f32 v14, v71\n"
    "v_max_f32_dpp v76, v76, v76 row_ror:8 row_mask:0xf bank_mask:0xf\n"
    "v_cos_f32 v248, v14\n"
    "v_sin_f32 v249, v14\n"
    "v_max_f32_dpp v76, v76, v76 row_ror:4 row_mask:0xf bank_mask:0xf\n"
    "v_mul_f32 v248, v13, v248\n"
    "v_mul_f32 v249, v13, v249\n"
    "v_max_f32_dpp v76, v76, v76 row_ror:2 row_mask:0xf bank_mask:0xf\n"
    "v_mul_f32 v25, 0x43000000, v70\n"
    "v_mul_f32 v26, 0x43000000, v71\n"
    "v_max_f32_dpp v76, v76, v76 row_ror:1 row_mask:0xf bank_mask:0xf\n"
    "v_exp_f32 v25, v25\n"
    "v_fract_f32 v26, v26\n"
    "v_mov_b32 v77, v76\n"
    "v_cos_f32 v252, v26\n"
    "v_sin_f32 v253, v26\n"
    "v_permlane16_swap_b32 v76, v77\n"
    "v_max_f32 v76, v76, v77\n"
    "v_mov_b32 v77, v76\n"
    "v_mul_f32 v252, v25, v252\n"
    "v_mul_f32 v253, v25, v253\n"
    "v_permlane32_swap_b32 v76, v77\n"
    "v_max_f32 v76, v76, v77\n"
    "v_frexp_exp_i32_f32 v76, v76\n"
    "v_sub_u32 v76, 6, v76\n"
    "v_max_i32 v76, -120, v76\n"
    "v_min_i32 v76, 120, v76\n"
    "v_ldexp_f32 v254, v72, v76\n"
    "v_ldexp_f32 v255, v74, v76\n"
    "v_sub_u32 v76, 0, v76\n"
    "v_ldexp_f32 v76, v68, v76\n"
    "v_pk_mul_f32 v[78:79], v[248:249], v[248:249] op_sel_hi:[0,1]\n"
    "v_pk_fma_f32 v[82:83], v[248:249], v[248:249], v[78:79] op_sel:[1,1,0] op_sel_hi:[1,0,1] neg_lo:[1,0,0]\n"
    "v_readfirstlane_b32 s76, v76\n"
    "v_pk_mul_f32 v[80:81], v[82:83], v[248:249] op_sel_hi:[0,1]\n"
    "v_pk_fma_f32 v[84:85], v[82:83], v[248:249], v[80:81] op_sel:[1,1,0] op_sel_hi:[1,0,1] neg_lo:[1,0,0]\n"
    "v_pk_mul_f32 v[78:79], v[82:83], v[82:83] op_sel_hi:[0,1]\n"
    "v_pk_fma_f32 v[86:87], v[82:83], v[82:83], v[78:79] op_sel:[1,1,0] op_sel_hi:[1,0,1] neg_lo:[1,0,0]\n"
    "v_pk_mul_f32 v[80:81], v[86:87], v[248:249] op_sel_hi:[0,1]\n"
    "v_pk_fma_f32 v[88:89], v[86:87], v[248:249], v[80:81] op_sel:[1,1,0] op_sel_hi:[1,0,1] neg_lo:[1,0,0]\n"
    "v_pk_mul_f32 v[78:79], v[86:87], v[82:83] op_sel_hi:[0,1]\n"
    "v_pk_fma_f32 v[90:91], v[86:87], v[82:83], v[78:79] op_sel:[1,1,0] op_sel_hi:[1,0,1] neg_lo:[1,0,0]\n"
    "v_pk_mul_f32 v[80:81], v[86:87], v[84:85] op_sel_hi:[0,1]\n"
    "v_pk_fma_f32 v[92:93], v[86:87], v[84:85], v[80:81] op_sel:[1,1,0] op_sel_hi:[1,0,1] neg_lo:[1,0,0]\n"
    "v_pk_mul_f32 v[78:79], v[86:87], v[86:87] op_sel_hi:[0,1] neg_hi:[1,0]\n"
    "v_pk_fma_f32 v[250:251], v[86:87], v[86:87], v[78:79] op_sel:[1,1,0] op_sel_hi:[1,0,1] neg_lo:[1,0,0] neg_hi:[1,0,0]\n"
    "v_cvt_pk_f16_f32 v26, v248, v249\n"
    "v_cvt_pk_f16_f32 v94, v82, v83\n"
    "v_cvt_pk_f16_f32 v95, v84, v85\n"
    "v_cvt_pk_f16_f32 v96, v86, v87\n"
    "v_cvt_pk_f16_f32 v97, v88, v89\n"
    "v_cvt_pk_f16_f32 v98, v90, v91\n"
    "v_cvt_pk_f16_f32 v99, v92, v93\n"
    "v_mov_b32 v2, 0x00003c00\n"
    "ds_write_addtid_b32 v2 offset:21648\n"
    "v_cvt_pk_f16_f32 v100, v250, v251\n"
    "ds_write_addtid_b32 v100 offset:22176\n"
    "v_cvt_pk_f16_f32 v110, v254, v255\n"
    "v_pk_mul_f32 v[80:81], v[254:255], v[252:253] op_sel_hi:[0,1]\n"
    "v_pk_fma_f32 v[254:255], v[254:255], v[252:253], v[80:81] op_sel:[1,1,0] op_sel_hi:[1,0,1] neg_lo:[1,0,0]\n"
    "ds_write_addtid_b32 v110 offset:0\n"
    "v_pk_mul_f16 v14, v110, v26 op_sel_hi:[0,1]\n"
    "v_pk_fma_f16 v14, v110, v26, v14 op_sel:[1,1,0] op_sel_hi:[1,0,1] neg_lo:[1,0,0]\n"
    "ds_write_addtid_b32 v14 offset:528\n"
    "v_pk_mul_f16 v25, v110, v94 op_sel_hi:[0,1]\n"
    "v_pk_fma_f16 v25, v110, v94, v25 op_sel:[1,1,0] op_sel_hi:[1,0,1] neg_lo:[1,0,0]\n"
    "ds_write_addtid_b32 v25 offset:1056\n"
    "v_pk_mul_f16 v13, v110, v95 op_sel_hi:[0,1]\n"
    "v_pk_fma_f16 v13, v110, v95, v13 op_sel:[1,1,0] op_sel_hi:[1,0,1] neg_lo:[1,0,0]\n"
    "ds_write_addtid_b32 v13 offset:1584\n"
    "v_pk_mul_f16 v14, v110, v96 op_sel_hi:[0,1]\n"
    "v_pk_fma_f16 v14, v110, v96, v14 op_sel:[1,1,0] op_sel_hi:[1,0,1] neg_lo:[1,0,0]\n"
    "ds_write_addtid_b32 v14 offset:2112\n"
    "v_pk_mul_f16 v25, v110, v97 op_sel_hi:[0,1]\n"
    "v_pk_fma_f16 v25, v110, v97, v25 op_sel:[1,1,0] op_sel_hi:[1,0,1] neg_lo:[1,0,0]\n"
    "ds_write_addtid_b32 v25 offset:2640\n"
    "v_pk_mul_f16 v13, v110, v98 op_sel_hi:[0,1]\n"
    "v_pk_fma_f16 v13, v110, v98, v13 op_sel:[1,1,0] op_sel_hi:[1,0,1] neg_lo:[1,0,0]\n"
    "ds_write_addtid_b32 v13 offset:3168\n"
    "v_pk_mul_f16 v14, v110, v99 op_sel_hi:[0,1]\n"
    "v_pk_fma_f16 v14, v110, v99, v14 op_sel:[1,1,0] op_sel_hi:[1,0,1] neg_lo:[1,0,0]\n"
    "ds_write_addtid_b32 v14 offset:3696\n"
    "v_pk_mul_f32 v[78:79], v[250:251], v[250:251] op_sel_hi:[0,1]\n"
    "v_pk_fma_f32 v[108:109], v[250:251], v[250:251], v[78:79] op_sel:[1,1,0] op_sel_hi:[1,0,1] neg_lo:[1,0,0]\n"
    "v_cvt_pk_f16_f32 v101, v108, v109\n"
    "ds_write_addtid_b32 v101 offset:22704\n"
    "v_cvt_pk_f16_f32 v110, v254, v255\n"
    "v_pk_mul_f32 v[80:81], v[254:255], v[252:253] op_sel_hi:[0,1]\n"
    "v_pk_fma_f32 v[254:255], v[254:255], v[252:253], v[80:81] op_sel:[1,1,0] op_sel_hi:[1,0,1] neg_lo:[1,0,0]\n"
    "ds_write_addtid_b32 v110 offset:4224\n"
    "v_pk_mul_f16 v25, v110, v26 op_sel_hi:[0,1]\n"
    "v_pk_fma_f16 v25, v110, v26, v25 op_sel:[1,1,0] op_sel_hi:[1,0,1] neg_lo:[1,0,0]\n"
    "ds_write_addtid_b32 v25 offset:4752\n"
    "v_pk_mul_f16 v13, v110, v94 op_sel_hi:[0,1]\n"
    "v_pk_fma_f16 v13, v110, v94, v13 op_sel:[1,1,0] op_sel_hi:[1,0,1] neg_lo:[1,0,0]\n"
    "ds_write_addtid_b32 v13 offset:5280\n"
    "v_pk_mul_f16 v14, v110, v95 op_sel_hi:[0,1]\n"
    "v_pk_fma_f16 v14, v110, v95, v14 op_sel:[1,1,0] op_sel_hi:[1,0,1] neg_lo:[1,0,0]\n"
    "ds_write_addtid_b32 v14 offset:5808\n"
    "v_pk_mul_f16 v25, v110, v96 op_sel_hi:[0,1]\n"
    "v_pk_fma_f16 v25, v110, v96, v25 op_sel:[1,1,0] op_sel_hi:[1,0,1] neg_lo:[1,0,0]\n"
    "ds_write_addtid_b32 v25 offset:6336\n"
    "v_pk_mul_f16 v13, v110, v97 op_sel_hi:[0,1]\n"
    "v_pk_fma_f16 v13, v110, v97, v13 op_sel:[1,1,0] op_sel_hi:[1,0,1] neg_lo:[1,0,0]\n"
    "ds_write_addtid_b32 v13 offset:6864\n"
    "v_pk_mul_f16 v14, v110, v98 op_sel_hi:[0,1]\n"
    "v_pk_fma_f16 v14, v110, v98, v14 op_sel:[1,1,0] op_sel_hi:[1,0,1] neg_lo:[1,0,0]\n"
    "ds_write_addtid_b32 v14 offset:7392\n"
    "v_pk_mul_f16 v25, v110, v99 op_sel_hi:[0,1]\n"
    "v_pk_fma_f16 v25, v110, v99, v25 op_sel:[1,1,0] op_sel_hi:[1,0,1] neg_lo:[1,0,0]\n"
    "ds_write_addtid_b32 v25 offset:7920\n"
    "v_pk_mul_f32 v[78:79], v[108:109], v[250:251] op_sel_hi:[0,1]\n"
    "v_pk_fma_f32 v[108:109], v[108:109], v[250:251], v[78:79] op_sel:[1,1,0] op_sel_hi:[1,0,1] neg_lo:[1,0,0]\n"
    "v_cvt_pk_f16_f32 v102, v108, v109\n"
    "ds_write_addtid_b32 v102 offset:23232\n"
    "v_cvt_pk_f16_f32 v110, v254, v255\n"
    "v_pk_mul_f32 v[80:81], v[254:255], v[252:253] op_sel_hi:[0,1]\n"
    "v_pk_fma_f32 v[254:255], v[254:255], v[252:253], v[80:81] op_sel:[1,1,0] op_sel_hi:[1,0,1] neg_lo:[1,0,0]\n"
    "ds_write_addtid_b32 v110 offset:8448\n"
    "v_pk_mul_f16 v13, v110, v94 op_sel_hi:[0,1]\n"
    "v_pk_fma_f16 v13, v110, v94, v13 op_sel:[1,1,0] op_sel_hi:[1,0,1] neg_lo:[1,0,0]\n"
    "ds_write_addtid_b32 v13 offset:8976\n"
    "v_pk_mul_f16 v14, v110, v96 op_sel_hi:[0,1]\n"
    "v_pk_fma_f16 v14, v110, v96, v14 op_sel:[1,1,0] op_sel_hi:[1,0,1] neg_lo:[1,0,0]\n"
    "ds_write_addtid_b32 v14 offset:9504\n"
    "v_pk_mul_f16 v25, v110, v98 op_sel_hi:[0,1]\n"
    "v_pk_fma_f16 v25, v110, v98, v25 op_sel:[1,1,0] op_sel_hi:[1,0,1] neg_lo:[1,0,0]\n"
    "ds_write_addtid_b32 v25 offset:10032\n"
    "v_pk_mul_f32 v[78:79], v[108:109], v[250:251] op_sel_hi:[0,1]\n"
    "v_pk_fma_f32 v[108:109], v[108:109], v[250:251], v[78:79] op_sel:[1,1,0] op_sel_hi:[1,0,1] neg_lo:[1,0,0]\n"
    "v_cvt_pk_f16_f32 v103, v108, v109\n"
    "ds_write_addtid_b32 v103 offset:23760\n"
    "v_cvt_pk_f16_f32 v110, v254, v255\n"
    "v_pk_mul_f32 v[80:81], v[254:255], v[252:253] op_sel_hi:[0,1]\n"
    "v_pk_fma_f32 v[254:255], v[254:255], v[252:253], v[80:81] op_sel:[1,1,0] op_sel_hi:[1,0,1] neg_lo:[1,0,0]\n"
    "ds_write_addtid_b32 v110 offset:10560\n"
    "v_pk_mul_f16 v13, v110, v94 op_sel_hi:[0,1]\n"
    "v_pk_fma_f16 v13, v110, v94, v13 op_sel:[1,1,0] op_sel_hi:[1,0,1] neg_lo:[1,0,0]\n"
    "ds_write_addtid_b32 v13 offset:11088\n"
    "v_pk_mul_f16 v14, v110, v96 op_sel_hi:[0,1]\n"
    "v_pk_fma_f16 v14, v110, v96, v14 op_sel:[1,1,0] op_sel_hi:[1,0,1] neg_lo:[1,0,0]\n"
    "ds_write_addtid_b32 v14 offset:11616\n"
    "v_pk_mul_f16 v25, v110, v98 op_sel_hi:[0,1]\n"
    "v_pk_fma_f16 v25, v110, v98, v25 op_sel:[1,1,0] op_sel_hi:[1,0,1] neg_lo:[1,0,0]\n"
    "ds_write_addtid_b32 v25 offset:12144\n"
    "v_pk_mul_f32 v[78:79], v[108:109], v[250:251] op_sel_hi:[0,1]\n"
    "v_pk_fma_f32 v[108:109], v[108:109], v[250:251], v[78:79] op_sel:[1,1,0] op_sel_hi:[1,0,1] neg_lo:[1,0,0]\n"
    "v_cvt_pk_f16_f32 v104, v108, v109\n"
    "ds_write_addtid_b32 v104 offset:24288\n"
    "v_cvt_pk_f16_f32 v110, v254, v255\n"
    "v_pk_mul_f32 v[80:81], v[254:255], v[252:253] op_sel_hi:[0,1]\n"
    "v_pk_fma_f32 v[254:255], v[254:255], v[252:253], v[80:81] op_sel:[1,1,0] op_sel_hi:[1,0,1] neg_lo:[1,0,0]\n"
    "ds_write_addtid_b32 v110 offset:12672\n"
    "v_pk_mul_f16 v13, v110, v96 op_sel_hi:[0,1]\n"
    "v_pk_fma_f16 v13, v110, v96, v13 op_sel:[1,1,0] op_sel_hi:[1,0,1] neg_lo:[1,0,0]\n"
    "ds_write_addtid_b32 v13 offset:13200\n"
    "v_pk_mul_f32 v[78:79], v[108:109], v[250:251] op_sel_hi:[0,1]\n"
    "v_pk_fma_f32 v[108:109], v[108:109], v[250:251], v[78:79] op_sel:[1,1,0] op_sel_hi:[1,0,1] neg_lo:[1,0,0]\n"
    "v_cvt_pk_f16_f32 v105, v108, v109\n"
    "ds_write_addtid_b32 v105 offset:24816\n"
    "v_cvt_pk_f16_f32 v110, v254, v255\n"
    "v_pk_mul_f32 v[80:81], v[254:255], v[252:253] op_sel_hi:[0,1]\n"
    "v_pk_fma_f32 v[254:255], v[254:255], v[252:253], v[80:81] op_sel:[1,1,0] op_sel_hi:[1,0,1] neg_lo:[1,0,0]\n"
    "ds_write_addtid_b32 v110 offset:13728\n"
    "v_pk_mul_f16 v14, v110, v96 op_sel_hi:[0,1]\n"
    "v_pk_fma_f16 v14, v110, v96, v14 op_sel:[1,1,0] op_sel_hi:[1,0,1] neg_lo:[1,0,0]\n"
    "ds_write_addtid_b32 v14 offset:14256\n"
    "v_pk_mul_f32 v[78:79], v[108:109], v[250:251] op_sel_hi:[0,1]\n"
    "v_pk_fma_f32 v[108:109], v[108:109], v[250:251], v[78:79] op_sel:[1,1,0] op_sel_hi:[1,0,1] neg_lo:[1,0,0]\n"
    "v_cvt_pk_f16_f32 v106, v108, v109\n"
    "ds_write_addtid_b32 v106 offset:25344\n"
    "v_cvt_pk_f16_f32 v110, v254, v255\n"
    "v_pk_mul_f32 v[80:81], v[254:255], v[252:253] op_sel_hi:[0,1]\n"
    "v_pk_fma_f32 v[254:255], v[254:255], v[252:253], v[80:81] op_sel:[1,1,0] op_sel_hi:[1,0,1] neg_lo:[1,0,0]\n"
    "ds_write_addtid_b32 v110 offset:14784\n"
    "v_pk_mul_f16 v25, v110, v96 op_sel_hi:[0,1]\n"
    "v_pk_fma_f16 v25, v110, v96, v25 op_sel:[1,1,0] op_sel_hi:[1,0,1] neg_lo:[1,0,0]\n"
    "ds_write_addtid_b32 v25 offset:15312\n"
    "v_pk_mul_f32 v[78:79], v[108:109], v[250:251] op_sel_hi:[0,1]\n"
    "v_pk_fma_f32 v[108:109], v[108:109], v[250:251], v[78:79] op_sel:[1,1,0] op_sel_hi:[1,0,1] neg_lo:[1,0,0]\n"
    "v_cvt_pk_f16_f32 v107, v108, v109\n"
    "ds_write_addtid_b32 v107 offset:25872\n"
    "v_cvt_pk_f16_f32 v110, v254, v255\n"
    "v_pk_mul_f32 v[80:81], v[254:255], v[252:253] op_sel_hi:[0,1]\n"
    "v_pk_fma_f32 v[254:255], v[254:255], v[252:253], v[80:81] op_sel:[1,1,0] op_sel_hi:[1,0,1] neg_lo:[1,0,0]\n"
    "ds_write_addtid_b32 v110 offset:15840\n"
    "v_pk_mul_f16 v13, v110, v96 op_sel_hi:[0,1]\n"
    "v_pk_fma_f16 v13, v110, v96, v13 op_sel:[1,1,0] op_sel_hi:[1,0,1] neg_lo:[1,0,0]\n"
    "ds_write_addtid_b32 v13 offset:16368\n"
    "v_pk_mul_f16 v14, v107, v100 op_sel_hi:[0,1]\n"
    "v_pk_fma_f16 v14, v107, v100, v14 op_sel:[1,1,0] op_sel_hi:[1,0,1] neg_lo:[1,0,0]\n"
    "ds_write_addtid_b32 v14 offset:26400\n"
    "v_cvt_pk_f16_f32 v110, v254, v255\n"
    "v_pk_mul_f32 v[80:81], v[254:255], v[252:253] op_sel_hi:[0,1]\n"
    "v_pk_fma_f32 v[254:255], v[254:255], v[252:253], v[80:81] op_sel:[1,1,0] op_sel_hi:[1,0,1] neg_lo:[1,0,0]\n"
    "ds_write_addtid_b32 v110 offset:16896\n"
    "v_pk_mul_f16 v25, v107, v101 op_sel_hi:[0,1]\n"
    "v_pk_fma_f16 v25, v107, v101, v25 op_sel:[1,1,0] op_sel_hi:[1,0,1] neg_lo:[1,0,0]\n"
    "ds_write_addtid_b32 v25 offset:26928\n"
    "v_cvt_pk_f16_f32 v110, v254, v255\n"
    "v_pk_mul_f32 v[80:81], v[254:255], v[252:253] op_sel_hi:[0,1]\n"
    "v_pk_fma_f32 v[254:255], v[254:255], v[252:253], v[80:81] op_sel:[1,1,0] op_sel_hi:[1,0,1] neg_lo:[1,0,0]\n"
    "ds_write_addtid_b32 v110 offset:17424\n"
    "v_pk_mul_f16 v13, v107, v102 op_sel_hi:[0,1]\n"
    "v_pk_fma_f16 v13, v107, v102, v13 op_sel:[1,1,0] op_sel_hi:[1,0,1] neg_lo:[1,0,0]\n"
    "ds_write_addtid_b32 v13 offset:27456\n"
    "v_cvt_pk_f16_f32 v110, v254, v255\n"
    "v_pk_mul_f32 v[80:81], v[254:255], v[252:253] op_sel_hi:[0,1]\n"
    "v_pk_fma_f32 v[254:255], v[254:255], v[252:253], v[80:81] op_sel:[1,1,0] op_sel_hi:[1,0,1] neg_lo:[1,0,0]\n"
    "ds_write_addtid_b32 v110 offset:17952\n"
    "v_pk_mul_f16 v14, v107, v103 op_sel_hi:[0,1]\n"
    "v_pk_fma_f16 v14, v107, v103, v14 op_sel:[1,1,0] op_sel_hi:[1,0,1] neg_lo:[1,0,0]\n"
    "ds_write_addtid_b32 v14 offset:27984\n"
    "v_cvt_pk_f16_f32 v110, v254, v255\n"
    "v_pk_mul_f32 v[80:81], v[254:255], v[252:253] op_sel_hi:[0,1]\n"
    "v_pk_fma_f32 v[254:255], v[254:255], v[252:253], v[80:81] op_sel:[1,1,0] op_sel_hi:[1,0,1] neg_lo:[1,0,0]\n"
    "ds_write_addtid_b32 v110 offset:18480\n"
    "v_pk_mul_f16 v25, v107, v104 op_sel_hi:[0,1]\n"
    "v_pk_fma_f16 v25, v107, v104, v25 op_sel:[1,1,0] op_sel_hi:[1,0,1] neg_lo:[1,0,0]\n"
    "ds_write_addtid_b32 v25 offset:28512\n"
    "v_cvt_pk_f16_f32 v110, v254, v255\n"
    "v_pk_mul_f32 v[80:81], v[254:255], v[252:253] op_sel_hi:[0,1]\n"
    "v_pk_fma_f32 v[254:255], v[254:255], v[252:253], v[80:81] op_sel:[1,1,0] op_sel_hi:[1,0,1] neg_lo:[1,0,0]\n"
    "ds_write_addtid_b32 v110 offset:19008\n"
    "v_pk_mul_f16 v13, v107, v105 op_sel_hi:[0,1]\n"
    "v_pk_fma_f16 v13, v107, v105, v13 op_sel:[1,1,0] op_sel_hi:[1,0,1] neg_lo:[1,0,0]\n"
    "ds_write_addtid_b32 v13 offset:29040\n"
    "v_cvt_pk_f16_f32 v110, v254, v255\n"
    "v_pk_mul_f32 v[80:81], v[254:255], v[252:253] op_sel_hi:[0,1]\n"
    "v_pk_fma_f32 v[254:255], v[254:255], v[252:253], v[80:81] op_sel:[1,1,0] op_sel_hi:[1,0,1] neg_lo:[1,0,0]\n"
    "ds_write_addtid_b32 v110 offset:19536\n"
    "v_pk_mul_f16 v14, v107, v106 op_sel_hi:[0,1]\n"
    "v_pk_fma_f16 v14, v107, v106, v14 op_sel:[1,1,0] op_sel_hi:[1,0,1] neg_lo:[1,0,0]\n"
    "ds_write_addtid_b32 v14 offset:29568\n"
    "v_cvt_pk_f16_f32 v110, v254, v255\n"
    "v_pk_mul_f32 v[80:81], v[254:255], v[252:253] op_sel_hi:[0,1]\n"
    "v_pk_fma_f32 v[254:255], v[254:255], v[252:253], v[80:81] op_sel:[1,1,0] op_sel_hi:[1,0,1] neg_lo:[1,0,0]\n"
    "ds_write_addtid_b32 v110 offset:20064\n"
    "v_cvt_pk_f16_f32 v110, v254, v255\n"
    "v_pk_mul_f32 v[80:81], v[254:255], v[252:253] op_sel_hi:[0,1]\n"
    "v_pk_fma_f32 v[254:255], v[254:255], v[252:253], v[80:81] op_sel:[1,1,0] op_sel_hi:[1,0,1] neg_lo:[1,0,0]\n"
    "ds_write_addtid_b32 v110 offset:20592\n"
    "v_cvt_pk_f16_f32 v110, v254, v255\n"
    "ds_write_addtid_b32 v110 offset:21120\n"
    "buffer_store_dwordx4 v[184:187], v27, s[32:35], s4 offen sc1\n"
    "buffer_store_dwordx4 v[188:191], v29, s[32:35], s4 offen sc1\n"
    "buffer_store_dwordx4 v[192:195], v27, s[32:35], s5 offen sc1\n"
    "buffer_store_dwordx4 v[196:199], v29, s[32:35], s5 offen sc1\n"
    "s_mov_b64 exec, s[8:9]\n"
    "buffer_store_dwordx4 v[64:67], v28, s[32:35], s6 offen sc1\n"
    "s_mov_b64 exec, -1\n"
    "s_mov_b32 s77, s76\n"
    "s_waitcnt vmcnt(9)\n"
    "v_swap_b32 v233, v236\n"
    "v_swap_b32 v234, v236\n"
    "v_swap_b32 v235, v237\n"
    "v_swap_b32 v237, v238\n"
    "ds_write_b128 v39, v[232:235] offset:0\n"
    "v_lshrrev_b32 v2, 1, v12\n"
    "v_and_b32 v3, 1, v12\n"
    "v_lshlrev_b32 v4, 7, v2\n"
    "v_lshl_add_u32 v4, v3, 2, v4\n"
    "v_lshlrev_b32 v5, 7, v12\n"
    "v_add_u32 v5, 256, v5\n"
    "v_lshlrev_b32 v3, 8, v12\n"
    "v_cmp_lt_u32 vcc, 1, v12\n"
    "v_mov_b32 v31, 2.0\n"
    "ds_write_b128 v39, v[236:239] offset:16\n"
    "v_mov_b32 v32, 4.0\n"
    "v_mov_b32 v30, 0x43000000\n"
    "v_cndmask_b32 v5, v5, v3, vcc\n"
    "v_cndmask_b32 v31, v31, v32, vcc\n"
    "s_waitcnt vmcnt(7)\n"
    "v_swap_b32 v241, v244\n"
    "v_swap_b32 v242, v244\n"
    "v_swap_b32 v243, v245\n"
    "v_swap_b32 v245, v246\n"
    "ds_write_b128 v39, v[240:243] offset:9216\n"
    "v_cndmask_b32 v32, v32, v30, vcc\n"
    "s_cmp_eq_u32 s60, 0\n"
    "s_cselect_b64 vcc, -1, 0\n"
    "v_cndmask_b32 v5, v5, v4, vcc\n"
    "v_cndmask_b32 v31, v31, 1.0, vcc\n"
    "v_cndmask_b32 v32, v32, 2.0, vcc\n"
    "v_lshl_add_u32 v5, v11, 3, v5\n"
    "v_cvt_f32_u32 v30, v5\n"
    "v_add_f32 v2, v30, v31\n"
    "ds_write_b128 v39, v[244:247] offset:9232\n"
    "v_add_f32 v3, v30, v32\n"
    "v_add_f32 v4, v2, v32\n"
    "v_cvt_pk_bf16_f32 v2, v30, v2\n"
    "v_cvt_pk_bf16_f32 v3, v3, v4\n"
    "v_lshlrev_b32_sdwa v34, 2, v2 dst_sel:DWORD dst_unused:UNUSED_PAD src0_sel:DWORD src1_sel:WORD_0\n"
    "v_lshlrev_b32_sdwa v35, 2, v2 dst_sel:DWORD dst_unused:UNUSED_PAD src0_sel:DWORD src1_sel:WORD_1\n"
    "v_lshlrev_b32_sdwa v36, 2, v3 dst_sel:DWORD dst_unused:UNUSED_PAD src0_sel:DWORD src1_sel:WORD_0\n"
    "v_lshlrev_b32_sdwa v37, 2, v3 dst_sel:DWORD dst_unused:UNUSED_PAD src0_sel:DWORD src1_sel:WORD_1\n"
    "v_max_u32 v34, 65020, v34\n"
    "s_waitcnt vmcnt(6)\n"
    "ds_write_b128 v40, v[224:227] offset:18432\n"
    "s_lshl_b32 s67, s59, 1\n"
    "v_add_u32 v34, s67, v34\n"
    "v_add_u32 v35, s67, v35\n"
    "v_add_u32 v36, s67, v36\n"
    "v_add_u32 v37, s67, v37\n"
    "s_add_u32 s67, s67, 70144\n"
    "v_lshl_add_u32 v3, v12, 6, v11\n"
    "v_lshl_add_u32 v33, v3, 2, s67\n"
    "s_mul_i32 s67, s59, 30096\n"
    "s_waitcnt vmcnt(5)\n"
    "ds_write_b128 v40, v[228:231] offset:18560\n"
    "v_mul_u32_u24 v223, 528, v11\n"
    "v_lshl_add_u32 v223, v12, 4, v223\n"
    "v_add_u32 v223, s67, v223\n"
    "s_mul_i32 s68, s60, 8448\n"
    "v_add_u32 v38, s68, v223\n"
    "s_waitcnt lgkmcnt(0)\n"
    "s_barrier\n"
    "ds_read_b64_tr_b16 v[232:233], v41 offset:0\n"
    "ds_read_b64_tr_b16 v[234:235], v41 offset:4608\n"
    "ds_read_b64_tr_b16 v[236:237], v41 offset:9216\n"
    "ds_read_b64_tr_b16 v[238:239], v41 offset:13824\n"
    "ds_read_b64_tr_b16 v[240:241], v41 offset:32\n"
    "ds_read_b64_tr_b16 v[242:243], v41 offset:4640\n"
    "ds_read_b64_tr_b16 v[244:245], v41 offset:9248\n"
    "ds_read_b64_tr_b16 v[246:247], v41 offset:13856\n"
    "ds_read_b64_tr_b16 v[224:225], v42 offset:18432\n"
    "ds_read_b64_tr_b16 v[226:227], v42 offset:19584\n"
    "ds_read_b64_tr_b16 v[228:229], v42 offset:18464\n"
    "ds_read_b64_tr_b16 v[230:231], v42 offset:19616\n"
    "s_waitcnt lgkmcnt(10)\n"
    "buffer_store_dwordx4 v[232:235], v24, s[36:39], s7 offen offset:0 sc1\n"
    "s_and_b32 s67, s57, 1\n"
    "s_lshl_b32 s67, s67, 3\n"
    "s_and_b32 s68, s58, 7\n"
    "s_add_u32 s61, s67, s68\n"
    "s_lshr_b32 s67, s57, 1\n"
    "s_lshl_b32 s67, s67, 2\n"
    "s_lshr_b32 s68, s58, 3\n"
    "s_add_u32 s62, s67, s68\n"
    "s_and_b32 s67, s57, 7\n"
    "s_lshl_b32 s67, s67, 2\n"
    "s_and_b32 s68, s58, 3\n"
    "s_add_u32 s67, s67, s68\n"
    "s_and_b32 s64, s67, 1\n"
    "s_lshr_b32 s63, s67, 1\n"
    "v_mfma_f32_16x16x32_bf16 v[184:187], v[232:235], v[58:61], 0\n"
    "s_waitcnt lgkmcnt(8)\n"
    "buffer_store_dwordx4 v[236:239], v24, s[36:39], s7 offen offset:1024 sc1\n"
    "s_lshr_b32 s67, s57, 3\n"
    "s_lshl_b32 s67, s67, 3\n"
    "s_lshr_b32 s68, s58, 2\n"
    "s_add_u32 s65, s67, s68\n"
    "v_mov_b32 v5, 0\n"
    "v_lshrrev_b32 v3, 7, v0\n"
    "v_mul_u32_u24 v3, 4672, v3\n"
    "v_and_b32 v4, 127, v0\n"
    "v_lshl_add_u32 v3, v4, 2, v3\n"
    "v_add_u32 v3, 98816, v3\n"
    "ds_write_b32 v3, v5\n"
    "v_mul_u32_u24 v3, 4160, v7\n"
    "v_lshl_add_u32 v3, v6, 2, v3\n"
    "v_add_u32 v3, 69632, v3\n"
    "v_mfma_f32_16x16x32_bf16 v[184:187], v[236:239], v[58:61], v[184:187]\n"
    "s_waitcnt lgkmcnt(7)\n"
    "buffer_store_dwordx4 v[240:243], v24, s[36:39], s10 offen offset:0 sc1\n"
    "ds_write_b32 v3, v5\n"
    "v_mov_b32 v3, 71168\n"
    "ds_write_b32 v3, v5 offset:51344\n"
    "v_mfma_f32_16x16x32_bf16 v[188:191], v[240:243], v[58:61], 0\n"
    "s_waitcnt lgkmcnt(7)\n"
    "buffer_store_dwordx4 v[244:247], v24, s[36:39], s10 offen offset:1024 sc1\n"
    "v_mfma_f32_16x16x32_bf16 v[188:191], v[244:247], v[58:61], v[188:191]\n"
    "s_mov_b32 exec_lo, 0x00010001\n"
    "s_mov_b32 exec_hi, 0x00010001\n"
    "buffer_store_dwordx4 v[184:187], v1, s[36:39], s12 offen offset:0 sc1\n"
    "s_nop 4\n"
    "buffer_store_dwordx4 v[188:191], v1, s[36:39], s12 offen offset:512 sc1\n"
    "s_mov_b64 exec, -1\n"
    "ds_read_b128 a[160:163], v223 offset:21648\n"
    "ds_read_b128 a[192:195], v38 offset:0\n"
    "ds_read_b128 a[224:227], v223 offset:16896\n"
    "v_lshlrev_b32 v2, 3, v0\n"
    "v_sub_u32 v2, 2047, v2\n"
    "v_cvt_f32_i32 v200, v2\n"
    "v_add_f32 v201, 0xbf800000, v200\n"
    "ds_read_b128 a[164:167], v223 offset:21712\n"
    "ds_read_b128 a[196:199], v38 offset:64\n"
    "s_waitcnt lgkmcnt(3)\n"
    "v_mfma_f32_16x16x32_f16 v[248:251], a[192:195], a[160:163], 0\n"
    "ds_read_b128 a[228:231], v223 offset:16960\n"
    "v_add_f32 v202, 0xc0000000, v200\n"
    "v_add_f32 v203, 0xc0400000, v200\n"
    "v_add_f32 v204, 0xc0800000, v200\n"
    "v_add_f32 v205, 0xc0a00000, v200\n"
    "s_waitcnt lgkmcnt(3)\n"
    "v_mfma_f32_16x16x32_f16 v[252:255], a[224:227], a[160:163], 0\n"
    "ds_read_b128 a[168:171], v223 offset:21776\n"
    "ds_read_b128 a[200:203], v38 offset:128\n"
    "s_waitcnt lgkmcnt(3)\n"
    "v_mfma_f32_16x16x32_f16 v[248:251], a[196:199], a[164:167], v[248:251]\n"
    "ds_read_b128 a[232:235], v223 offset:17024\n"
    "v_add_f32 v206, 0xc0c00000, v200\n"
    "v_add_f32 v207, 0xc0e00000, v200\n"
    "v_add_f32 v208, 0xc1000000, v200\n"
    "v_add_f32 v209, 0xc1100000, v200\n"
    "s_waitcnt lgkmcnt(3)\n"
    "v_mfma_f32_16x16x32_f16 v[252:255], a[228:231], a[164:167], v[252:255]\n"
    "ds_read_b128 a[172:175], v223 offset:21840\n"
    "ds_read_b128 a[204:207], v38 offset:192\n"
    "s_waitcnt lgkmcnt(3)\n"
    "v_mfma_f32_16x16x32_f16 v[248:251], a[200:203], a[168:171], v[248:251]\n"
    "ds_read_b128 a[236:239], v223 offset:17088\n"
    "v_add_f32 v210, 0xc1200000, v200\n"
    "v_cvt_pk_bf16_f32 v200, v200, v201\n"
    "v_cvt_pk_bf16_f32 v202, v202, v203\n"
    "v_cvt_pk_bf16_f32 v204, v204, v205\n"
    "s_waitcnt lgkmcnt(3)\n"
    "v_mfma_f32_16x16x32_f16 v[252:255], a[232:235], a[168:171], v[252:255]\n"
    "ds_read_b128 a[176:179], v223 offset:21904\n"
    "ds_read_b128 a[208:211], v38 offset:256\n"
    "s_waitcnt lgkmcnt(3)\n"
    "v_mfma_f32_16x16x32_f16 v[248:251], a[204:207], a[172:175], v[248:251]\n"
    "ds_read_b128 a[240:243], v223 offset:17152\n"
    "v_cvt_pk_bf16_f32 v206, v206, v207\n"
    "v_cvt_pk_bf16_f32 v208, v208, v209\n"
    "v_cvt_pk_bf16_f32 v210, v210, v210\n"
    "v_lshlrev_b32_sdwa v212, 2, v200 dst_sel:DWORD dst_unused:UNUSED_PAD src0_sel:DWORD src1_sel:WORD_0\n"
    "s_waitcnt lgkmcnt(3)\n"
    "v_mfma_f32_16x16x32_f16 v[252:255], a[236:239], a[172:175], v[252:255]\n"
    "ds_read_b128 a[180:183], v223 offset:21968\n"
    "ds_read_b128 a[212:215], v38 offset:320\n"
    "s_waitcnt lgkmcnt(3)\n"
    "v_mfma_f32_16x16x32_f16 v[248:251], a[208:211], a[176:179], v[248:251]\n"
    "ds_read_b128 a[244:247], v223 offset:17216\n"
    "v_lshlrev_b32_sdwa v213, 2, v200 dst_sel:DWORD dst_unused:UNUSED_PAD src0_sel:DWORD src1_sel:WORD_1\n"
    "v_lshlrev_b32_sdwa v214, 2, v202 dst_sel:DWORD dst_unused:UNUSED_PAD src0_sel:DWORD src1_sel:WORD_0\n"
    "v_lshlrev_b32_sdwa v215, 2, v202 dst_sel:DWORD dst_unused:UNUSED_PAD src0_sel:DWORD src1_sel:WORD_1\n"
    "v_lshlrev_b32_sdwa v216, 2, v204 dst_sel:DWORD dst_unused:UNUSED_PAD src0_sel:DWORD src1_sel:WORD_0\n"
    "s_waitcnt lgkmcnt(3)\n"
    "v_mfma_f32_16x16x32_f16 v[252:255], a[240:243], a[176:179], v[252:255]\n"
    "ds_read_b128 a[184:187], v223 offset:22032\n"
    "ds_read_b128 a[216:219], v38 offset:384\n"
    "s_waitcnt lgkmcnt(3)\n"
    "v_mfma_f32_16x16x32_f16 v[248:251], a[212:215], a[180:183], v[248:251]\n"
    "ds_read_b128 a[248:251], v223 offset:17280\n"
    "v_lshlrev_b32_sdwa v217, 2, v204 dst_sel:DWORD dst_unused:UNUSED_PAD src0_sel:DWORD src1_sel:WORD_1\n"
    "v_lshlrev_b32_sdwa v218, 2, v206 dst_sel:DWORD dst_unused:UNUSED_PAD src0_sel:DWORD src1_sel:WORD_0\n"
    "v_lshlrev_b32_sdwa v219, 2, v206 dst_sel:DWORD dst_unused:UNUSED_PAD src0_sel:DWORD src1_sel:WORD_1\n"
    "v_lshlrev_b32_sdwa v220, 2, v208 dst_sel:DWORD dst_unused:UNUSED_PAD src0_sel:DWORD src1_sel:WORD_0\n"
    "s_waitcnt lgkmcnt(3)\n"
    "v_mfma_f32_16x16x32_f16 v[252:255], a[244:247], a[180:183], v[252:255]\n"
    "ds_read_b128 a[188:191], v223 offset:22096\n"
    "ds_read_b128 a[220:223], v38 offset:448\n"
    "s_waitcnt lgkmcnt(3)\n"
    "v_mfma_f32_16x16x32_f16 v[248:251], a[216:219], a[184:187], v[248:251]\n"
    "ds_read_b128 a[252:255], v223 offset:17344\n"
    "v_lshlrev_b32_sdwa v221, 2, v208 dst_sel:DWORD dst_unused:UNUSED_PAD src0_sel:DWORD src1_sel:WORD_1\n"
    "v_lshlrev_b32_sdwa v222, 2, v210 dst_sel:DWORD dst_unused:UNUSED_PAD src0_sel:DWORD src1_sel:WORD_0\n"
    "v_max_u32 v219, 65020, v219\n"
    "v_min_u32 v220, 71168, v220\n"
    "s_waitcnt lgkmcnt(3)\n"
    "v_mfma_f32_16x16x32_f16 v[252:255], a[248:251], a[184:187], v[252:255]\n"
    "buffer_load_dwordx4 v[160:163], v46, s[32:35], s16 offen sc1\n"
    "buffer_load_dwordx4 v[164:167], v46, s[32:35], s17 offen sc1\n"
    "buffer_load_dwordx4 v[168:171], v46, s[32:35], s18 offen sc1\n"
    "buffer_load_dwordx4 v[172:175], v46, s[32:35], s19 offen sc1\n"
    "s_waitcnt lgkmcnt(1)\n"
    "v_mfma_f32_16x16x32_f16 v[248:251], a[220:223], a[188:191], v[248:251]\n"
    "v_min_u32 v221, 71168, v221\n"
    "v_min_u32 v222, 71168, v222\n"
    "s_waitcnt lgkmcnt(0)\n"
    "v_mfma_f32_16x16x32_f16 v[252:255], a[252:255], a[188:191], v[252:255]\n"
    "s_nop 3\n"
    "v_cvt_pk_f16_f32 v248, v248, v249\n"
    "v_cvt_pk_f16_f32 v250, v250, v251\n"
    "s_nop 1\n"
    "v_cvt_pk_f16_f32 v252, v252, v253\n"
    "v_cvt_pk_f16_f32 v254, v254, v255\n"
    "ds_write_b16 v34, v248 offset:51344\n"
    "ds_write_b16_d16_hi v35, v248 offset:51344\n"
    "ds_write_b16 v36, v250 offset:51344\n"
    "ds_write_b16_d16_hi v37, v250 offset:51344\n"
    "ds_write_b16 v33, v252 offset:51344\n"
    "ds_write_b16_d16_hi v33, v252 offset:51408\n"
    "ds_write_b16 v33, v254 offset:51472\n"
    "ds_write_b16_d16_hi v33, v254 offset:51536\n"
    "s_waitcnt vmcnt(4)\n"
    "s_waitcnt lgkmcnt(0)\n"
    "s_barrier\n"
    "s_add_u32 s67, s13, 655360\n"
    "s_mov_b64 exec, 3\n"
    "buffer_store_dwordx2 v[62:63], v44, s[32:35], s67 offen sc1\n"
    "s_mov_b64 exec, -1\n"
    "ds_read_b32 v244, v212 offset:51344\n"
    "ds_read_b32 v245, v213 offset:51344\n"
    "ds_read_b32 v246, v214 offset:51344\n"
    "ds_read_b32 v247, v215 offset:51344\n"
    "ds_read_b32 v248, v216 offset:51344\n"
    "ds_read_b32 v249, v217 offset:51344\n"
    "ds_read_b32 v250, v218 offset:51344\n"
    "ds_read_b32 v251, v219 offset:51344\n"
    "ds_read_b32 v252, v220 offset:51344\n"
    "ds_read_b32 v253, v221 offset:51344\n"
    "ds_read_b32 v254, v222 offset:51344\n"
    "s_waitcnt lgkmcnt(9)\n"
    "v_perm_b32 v184, v245, v244, s24\n"
    "s_waitcnt lgkmcnt(7)\n"
    "v_perm_b32 v185, v247, v246, s24\n"
    "s_waitcnt lgkmcnt(5)\n"
    "v_perm_b32 v186, v249, v248, s24\n"
    "s_waitcnt lgkmcnt(3)\n"
    "v_perm_b32 v187, v251, v250, s24\n"
    "ds_write_b128 v56, v[184:187] offset:0\n"
    "s_lshl_b32 s67, s56, 10\n"
    "s_add_u32 s67, s67, 99328\n"
    "v_lshl_add_u32 v48, v1, 2, s67\n"
    "v_lshlrev_b32 v57, 3, v0\n"
    "v_add_u32 v57, 108160, v57\n"
    "v_and_b32 v2, 3, v11\n"
    "v_sub_u32 v2, 3, v2\n"
    "v_mul_u32_u24 v2, 4160, v2\n"
    "v_lshl_add_u32 v2, v12, 4, v2\n"
    "v_perm_b32 v188, v246, v245, s24\n"
    "v_perm_b32 v189, v248, v247, s24\n"
    "v_perm_b32 v190, v250, v249, s24\n"
    "s_waitcnt lgkmcnt(3)\n"
    "v_perm_b32 v191, v252, v251, s24\n"
    "ds_write_b128 v56, v[188:191] offset:4160\n"
    "v_lshlrev_b32 v3, 3, v9\n"
    "v_sub_u32 v2, v2, v3\n"
    "s_mul_i32 s67, s59, 16640\n"
    "s_lshl_b32 s68, s60, 8\n"
    "s_sub_u32 s67, s67, s68\n"
    "s_add_u32 s67, s67, 65816\n"
    "v_add_u32 v49, s67, v2\n"
    "v_lshlrev_b32 v2, 5, v11\n"
    "v_lshl_add_u32 v3, v12, 4, v2\n"
    "v_perm_b32 v192, v247, v246, s24\n"
    "v_perm_b32 v193, v249, v248, s24\n"
    "v_perm_b32 v194, v251, v250, s24\n"
    "s_waitcnt lgkmcnt(3)\n"
    "v_perm_b32 v195, v253, v252, s24\n"
    "ds_write_b128 v56, v[192:195] offset:8320\n"
    "s_mul_i32 s69, s59, 4672\n"
    "s_sub_u32 s67, s69, s68\n"
    "s_add_u32 s67, s67, 99104\n"
    "v_add_u32 v50, s67, v3\n"
    "v_lshl_add_u32 v3, v12, 3, v2\n"
    "s_lshl_b32 s67, s60, 10\n"
    "s_add_u32 s68, s67, s69\n"
    "s_add_u32 s68, s68, 99328\n"
    "v_add_u32 v53, s68, v3\n"
    "v_perm_b32 v196, v248, v247, s24\n"
    "v_perm_b32 v197, v250, v249, s24\n"
    "v_perm_b32 v198, v252, v251, s24\n"
    "s_waitcnt lgkmcnt(3)\n"
    "v_perm_b32 v199, v254, v253, s24\n"
    "ds_write_b128 v56, v[196:199] offset:12480\n"
    "s_lshl_b32 s68, s60, 11\n"
    "s_add_u32 s69, s68, 108160\n"
    "v_lshl_add_u32 v54, v3, 1, s69\n"
    "s_lshl_b32 s69, s56, 12\n"
    "s_add_u32 s69, s69, 131072\n"
    "v_add_u32 v51, s69, v24\n"
    "s_lshl_b32 s69, s59, 13\n"
    "s_add_u32 s69, s69, s68\n"
    "s_add_u32 s69, s69, 131072\n"
    "v_perm_b32 v184, v245, v244, s25\n"
    "v_perm_b32 v185, v247, v246, s25\n"
    "v_perm_b32 v186, v249, v248, s25\n"
    "v_perm_b32 v187, v251, v250, s25\n"
    "ds_write_b128 v56, v[184:187] offset:16640\n"
    "v_xor_b32 v52, 4096, v51\n"
    "v_and_b32 v3, 1, v12\n"
    "v_lshl_add_u32 v2, v3, 9, v2\n"
    "v_lshrrev_b32 v3, 1, v12\n"
    "v_lshl_add_u32 v2, v3, 4, v2\n"
    "s_lshl_b32 s68, s66, 12\n"
    "s_add_u32 s67, s67, s68\n"
    "v_add_u32 v55, s67, v2\n"
    "s_mov_b32 s15, 16777216\n"
    "v_perm_b32 v188, v246, v245, s25\n"
    "v_perm_b32 v189, v248, v247, s25\n"
    "v_perm_b32 v190, v250, v249, s25\n"
    "v_perm_b32 v191, v252, v251, s25\n"
    "ds_write_b128 v56, v[188:191] offset:20800\n"
    "s_mul_i32 s67, s62, 14\n"
    "s_lshr_b32 s67, s67, 3\n"
    "s_lshl_b32 s67, s67, 8\n"
    "s_add_u32 s67, s67, 655360\n"
    "s_add_u32 s4, s67, s14\n"
    "v_min_u32 v10, 23, v1\n"
    "v_lshlrev_b32 v10, 5, v10\n"
    "s_lshl_b32 s67, s59, 13\n"
    "v_add_u32 v26, s67, v24\n"
    "v_perm_b32 v192, v247, v246, s25\n"
    "v_perm_b32 v193, v249, v248, s25\n"
    "v_perm_b32 v194, v251, v250, s25\n"
    "v_perm_b32 v195, v253, v252, s25\n"
    "ds_write_b128 v56, v[192:195] offset:24960\n"
    "v_xor_b32 v27, 4096, v26\n"
    "v_xor_b32 v28, 8192, v26\n"
    "v_xor_b32 v29, 12288, v26\n"
    "s_mul_i32 s67, s62, 14\n"
    "s_mul_i32 s68, s60, 7\n"
    "s_add_u32 s67, s67, s68\n"
    "s_mul_i32 s68, s59, 3\n"
    "s_add_u32 s5, s67, s68\n"
    "s_add_u32 s6, s67, 6\n"
    "v_perm_b32 v196, v248, v247, s25\n"
    "v_perm_b32 v197, v250, v249, s25\n"
    "v_perm_b32 v198, v252, v251, s25\n"
    "v_perm_b32 v199, v254, v253, s25\n"
    "ds_write_b128 v56, v[196:199] offset:29120\n"
    "s_lshl_b32 s40, s5, 14\n"
    "s_add_u32 s40, s40, 10485760\n"
    "s_add_u32 s41, s40, 16384\n"
    "s_add_u32 s42, s40, 32768\n"
    "s_lshl_b32 s43, s6, 14\n"
    "s_add_u32 s43, s43, 10485760\n"
    "s_waitcnt vmcnt(4)\n"
    "v_xor_b32 v2, s72, v161\n"
    "v_bitop3_b32 v2, v163, v62, v2 bitop3:0xbe\n"
    "s_waitcnt vmcnt(3)\n"
    "v_bitop3_b32 v2, v165, v62, v2 bitop3:0xbe\n"
    "v_bitop3_b32 v2, v167, v62, v2 bitop3:0xbe\n"
    "s_waitcnt vmcnt(2)\n"
    "v_bitop3_b32 v2, v169, v62, v2 bitop3:0xbe\n"
    "v_bitop3_b32 v2, v171, v62, v2 bitop3:0xbe\n"
    "s_waitcnt vmcnt(1)\n"
    "v_bitop3_b32 v2, v173, v62, v2 bitop3:0xbe\n"
    "v_bitop3_b32 v2, v175, v62, v2 bitop3:0xbe\n"
    "v_cmp_ne_u32 vcc, 0, v2\n"
    "s_nop 1\n"
    "s_cbranch_vccz .Lmk_gok_1\n"
    "s_mov_b32 s78, 0\n"
    ".Lmk_gspin_2:\n"
    "s_mov_b64 exec, vcc\n"
    "buffer_load_dwordx4 v[160:163], v46, s[32:35], s16 offen sc1\n"
    "buffer_load_dwordx4 v[164:167], v46, s[32:35], s17 offen sc1\n"
    "buffer_load_dwordx4 v[168:171], v46, s[32:35], s18 offen sc1\n"
    "buffer_load_dwordx4 v[172:175], v46, s[32:35], s19 offen sc1\n"
    "s_add_u32 s78, s78, 1\n"
    "s_waitcnt vmcnt(0)\n"
    "v_xor_b32 v2, s72, v161\n"
    "v_bitop3_b32 v2, v163, v62, v2 bitop3:0xbe\n"
    "v_bitop3_b32 v2, v165, v62, v2 bitop3:0xbe\n"
    "v_bitop3_b32 v2, v167, v62, v2 bitop3:0xbe\n"
    "v_bitop3_b32 v2, v169, v62, v2 bitop3:0xbe\n"
    "v_bitop3_b32 v2, v171, v62, v2 bitop3:0xbe\n"
    "v_bitop3_b32 v2, v173, v62, v2 bitop3:0xbe\n"
    "v_bitop3_b32 v2, v175, v62, v2 bitop3:0xbe\n"
    "v_cmp_ne_u32 vcc, 0, v2\n"
    "s_nop 1\n"
    "s_cbranch_vccz .Lmk_gfin_3\n"
    "s_cmp_lt_u32 s78, 20000\n"
    "s_cbranch_scc1 .Lmk_gspin_2\n"
    ".Lmk_gfin_3:\n"
    "s_mov_b64 exec, -1\n"
    ".Lmk_gok_1:\n"
    "buffer_load_dwordx4 v[240:243], v47, s[32:35], s20 offen sc1\n"
    "buffer_load_dwordx4 v[244:247], v47, s[32:35], s21 offen sc1\n"
    "buffer_load_dwordx4 v[248:251], v47, s[32:35], s22 offen sc1\n"
    "buffer_load_dwordx4 v[252:255], v47, s[32:35], s23 offen sc1\n"
    "v_perm_b32 v161, v162, v160, s24\n"
    "v_perm_b32 v163, v162, v160, s25\n"
    "ds_write_b32 v48, v161 offset:0\n"
    "ds_write_b32 v48, v163 offset:4672\n"
    "v_perm_b32 v165, v166, v164, s24\n"
    "v_perm_b32 v167, v166, v164, s25\n"
    "ds_write_b32 v48, v165 offset:256\n"
    "ds_write_b32 v48, v167 offset:4928\n"
    "v_perm_b32 v169, v170, v168, s24\n"
    "v_perm_b32 v171, v170, v168, s25\n"
    "ds_write_b32 v48, v169 offset:512\n"
    "ds_write_b32 v48, v171 offset:5184\n"
    "v_perm_b32 v173, v174, v172, s24\n"
    "v_perm_b32 v175, v174, v172, s25\n"
    "ds_write_b32 v48, v173 offset:768\n"
    "ds_write_b32 v48, v175 offset:5440\n"
    "s_waitcnt lgkmcnt(0)\n"
    "s_barrier\n"
    "buffer_load_dwordx2 v[14:15], v10, s[32:35], s4 offen sc1\n"
    "ds_read_b64 a[192:193], v49 offset:3776\n"
    "ds_read_b64 a[194:195], v49 offset:3784\n"
    "ds_read_b128 v[160:163], v50 offset:192\n"
    "ds_read_b64 a[196:197], v49 offset:3264\n"
    "ds_read_b64 a[198:199], v49 offset:3272\n"
    "ds_read_b128 v[164:167], v50 offset:704\n"
    "ds_read_b64 a[200:201], v49 offset:2752\n"
    "ds_read_b64 a[202:203], v49 offset:2760\n"
    "ds_read_b128 v[168:171], v50 offset:1216\n"
    "ds_read_b64 a[204:205], v49 offset:2240\n"
    "ds_read_b64 a[206:207], v49 offset:2248\n"
    "ds_read_b128 v[172:175], v50 offset:1728\n"
    "s_waitcnt lgkmcnt(9)\n"
    "v_mfma_f32_16x16x32_f16 v[128:131], a[192:195], v[160:163], 0\n"
    "ds_read_b64 a[208:209], v49 offset:3712\n"
    "ds_read_b64 a[210:211], v49 offset:3720\n"
    "s_waitcnt lgkmcnt(8)\n"
    "v_mfma_f32_16x16x32_f16 v[132:135], a[192:195], v[164:167], 0\n"
    "ds_read_b128 v[176:179], v50 offset:128\n"
    "v_mfma_f32_16x16x32_f16 v[132:135], a[196:199], v[160:163], v[132:135]\n"
    "ds_read_b64 a[212:213], v49 offset:3200\n"
    "v_mfma_f32_16x16x32_f16 v[136:139], a[196:199], v[164:167], 0\n"
    "ds_read_b64 a[214:215], v49 offset:3208\n"
    "s_waitcnt lgkmcnt(8)\n"
    "v_mfma_f32_16x16x32_f16 v[136:139], a[192:195], v[168:171], v[136:139]\n"
    "ds_read_b128 v[180:183], v50 offset:640\n"
    "v_mfma_f32_16x16x32_f16 v[140:143], a[196:199], v[168:171], 0\n"
    "ds_read_b64 a[216:217], v49 offset:2688\n"
    "ds_read_b64 a[218:219], v49 offset:2696\n"
    "v_mfma_f32_16x16x32_f16 v[136:139], a[200:203], v[160:163], v[136:139]\n"
    "ds_read_b128 v[184:187], v50 offset:1152\n"
    "v_mfma_f32_16x16x32_f16 v[140:143], a[200:203], v[164:167], v[140:143]\n"
    "ds_read_b64 a[220:221], v49 offset:2176\n"
    "s_waitcnt lgkmcnt(10)\n"
    "v_mfma_f32_16x16x32_f16 v[140:143], a[192:195], v[172:175], v[140:143]\n"
    "ds_read_b64 a[222:223], v49 offset:2184\n"
    "v_mfma_f32_16x16x32_f16 v[140:143], a[204:207], v[160:163], v[140:143]\n"
    "ds_read_b128 v[188:191], v50 offset:1664\n"
    "s_waitcnt lgkmcnt(9)\n"
    "v_mfma_f32_16x16x32_f16 v[128:131], a[208:211], v[176:179], v[128:131]\n"
    "ds_read_b64 a[224:225], v49 offset:3648\n"
    "ds_read_b64 a[226:227], v49 offset:3656\n"
    "s_waitcnt lgkmcnt(8)\n"
    "v_mfma_f32_16x16x32_f16 v[132:135], a[208:211], v[180:183], v[132:135]\n"
    "ds_read_b128 v[192:195], v50 offset:64\n"
    "v_mfma_f32_16x16x32_f16 v[132:135], a[212:215], v[176:179], v[132:135]\n"
    "ds_read_b64 a[228:229], v49 offset:3136\n"
    "v_mfma_f32_16x16x32_f16 v[136:139], a[212:215], v[180:183], v[136:139]\n"
    "ds_read_b64 a[230:231], v49 offset:3144\n"
    "s_waitcnt lgkmcnt(8)\n"
    "v_mfma_f32_16x16x32_f16 v[136:139], a[208:211], v[184:187], v[136:139]\n"
    "ds_read_b128 v[196:199], v50 offset:576\n"
    "v_mfma_f32_16x16x32_f16 v[140:143], a[212:215], v[184:187], v[140:143]\n"
    "ds_read_b64 a[232:233], v49 offset:2624\n"
    "ds_read_b64 a[234:235], v49 offset:2632\n"
    "v_mfma_f32_16x16x32_f16 v[136:139], a[216:219], v[176:179], v[136:139]\n"
    "ds_read_b128 v[200:203], v50 offset:1088\n"
    "v_mfma_f32_16x16x32_f16 v[140:143], a[216:219], v[180:183], v[140:143]\n"
    "ds_read_b64 a[236:237], v49 offset:2112\n"
    "s_waitcnt lgkmcnt(10)\n"
    "v_mfma_f32_16x16x32_f16 v[140:143], a[208:211], v[188:191], v[140:143]\n"
    "ds_read_b64 a[238:239], v49 offset:2120\n"
    "v_mfma_f32_16x16x32_f16 v[140:143], a[220:223], v[176:179], v[140:143]\n"
    "ds_read_b128 v[204:207], v50 offset:1600\n"
    "s_waitcnt lgkmcnt(9)\n"
    "v_mfma_f32_16x16x32_f16 v[128:131], a[224:227], v[192:195], v[128:131]\n"
    "ds_read_b64 a[240:241], v49 offset:3584\n"
    "ds_read_b64 a[242:243], v49 offset:3592\n"
    "s_waitcnt lgkmcnt(8)\n"
    "v_mfma_f32_16x16x32_f16 v[132:135], a[224:227], v[196:199], v[132:135]\n"
    "ds_read_b128 v[208:211], v50 offset:0\n"
    "v_mfma_f32_16x16x32_f16 v[132:135], a[228:231], v[192:195], v[132:135]\n"
    "ds_read_b64 a[244:245], v49 offset:3072\n"
    "v_mfma_f32_16x16x32_f16 v[136:139], a[228:231], v[196:199], v[136:139]\n"
    "ds_read_b64 a[246:247], v49 offset:3080\n"
    "s_waitcnt lgkmcnt(8)\n"
    "v_mfma_f32_16x16x32_f16 v[136:139], a[224:227], v[200:203], v[136:139]\n"
    "ds_read_b128 v[212:215], v50 offset:512\n"
    "v_mfma_f32_16x16x32_f16 v[140:143], a[228:231], v[200:203], v[140:143]\n"
    "ds_read_b64 a[248:249], v49 offset:2560\n"
    "ds_read_b64 a[250:251], v49 offset:2568\n"
    "v_mfma_f32_16x16x32_f16 v[136:139], a[232:235], v[192:195], v[136:139]\n"
    "ds_read_b128 v[216:219], v50 offset:1024\n"
    "v_mfma_f32_16x16x32_f16 v[140:143], a[232:235], v[196:199], v[140:143]\n"
    "ds_read_b64 a[252:253], v49 offset:2048\n"
    "s_waitcnt lgkmcnt(10)\n"
    "v_mfma_f32_16x16x32_f16 v[140:143], a[224:227], v[204:207], v[140:143]\n"
    "ds_read_b64 a[254:255], v49 offset:2056\n"
    "v_mfma_f32_16x16x32_f16 v[140:143], a[236:239], v[192:195], v[140:143]\n"
    "ds_read_b128 v[220:223], v50 offset:1536\n"
    "s_waitcnt lgkmcnt(9)\n"
    "v_mfma_f32_16x16x32_f16 v[128:131], a[240:243], v[208:211], v[128:131]\n"
    "ds_read_b64 a[160:161], v49 offset:1728\n"
    "ds_read_b64 a[162:163], v49 offset:1736\n"
    "s_waitcnt lgkmcnt(8)\n"
    "v_mfma_f32_16x16x32_f16 v[132:135], a[240:243], v[212:215], v[132:135]\n"
    "ds_read_b128 a[128:131], v50 offset:2240\n"
    "v_mfma_f32_16x16x32_f16 v[132:135], a[244:247], v[208:211], v[132:135]\n"
    "ds_read_b64 a[164:165], v49 offset:1216\n"
    "v_mfma_f32_16x16x32_f16 v[136:139], a[244:247], v[212:215], v[136:139]\n"
    "ds_read_b64 a[166:167], v49 offset:1224\n"
    "s_waitcnt lgkmcnt(8)\n"
    "v_mfma_f32_16x16x32_f16 v[136:139], a[240:243], v[216:219], v[136:139]\n"
    "ds_read_b128 a[132:135], v50 offset:2752\n"
    "v_mfma_f32_16x16x32_f16 v[140:143], a[244:247], v[216:219], v[140:143]\n"
    "ds_read_b64 a[168:169], v49 offset:704\n"
    "ds_read_b64 a[170:171], v49 offset:712\n"
    "v_mfma_f32_16x16x32_f16 v[136:139], a[248:251], v[208:211], v[136:139]\n"
    "ds_read_b128 a[136:139], v50 offset:3264\n"
    "v_mfma_f32_16x16x32_f16 v[140:143], a[248:251], v[212:215], v[140:143]\n"
    "ds_read_b64 a[172:173], v49 offset:192\n"
    "s_waitcnt lgkmcnt(10)\n"
    "v_mfma_f32_16x16x32_f16 v[140:143], a[240:243], v[220:223], v[140:143]\n"
    "ds_read_b64 a[174:175], v49 offset:200\n"
    "v_mfma_f32_16x16x32_f16 v[140:143], a[252:255], v[208:211], v[140:143]\n"
    "ds_read_b128 a[140:143], v50 offset:3776\n"
    "s_cmp_eq_u32 s60, 0\n"
    "s_cbranch_scc0 .Lmk_arm_4\n"
    "ds_write_b128 v51, v[136:139] offset:0\n"
    "s_nop 3\n"
    "ds_write_b128 v51, v[140:143] offset:1024\n"
    "s_branch .Lmk_join_5\n"
    ".Lmk_arm_4:\n"
    "ds_write_b128 v51, v[128:131] offset:0\n"
    "ds_write_b128 v51, v[132:135] offset:1024\n"
    ".Lmk_join_5:\n"
    "s_waitcnt vmcnt(4)\n"
    "v_xor_b32 v2, s72, v241\n"
    "v_bitop3_b32 v2, v243, v62, v2 bitop3:0xbe\n"
    "s_waitcnt vmcnt(3)\n"
    "v_bitop3_b32 v2, v245, v62, v2 bitop3:0xbe\n"
    "v_bitop3_b32 v2, v247, v62, v2 bitop3:0xbe\n"
    "s_waitcnt vmcnt(2)\n"
    "v_bitop3_b32 v2, v249, v62, v2 bitop3:0xbe\n"
    "v_bitop3_b32 v2, v251, v62, v2 bitop3:0xbe\n"
    "s_waitcnt vmcnt(1)\n"
    "v_bitop3_b32 v2, v253, v62, v2 bitop3:0xbe\n"
    "v_bitop3_b32 v2, v255, v62, v2 bitop3:0xbe\n"
    "v_cmp_ne_u32 vcc, 0, v2\n"
    "s_nop 1\n"
    "s_cbranch_vccz .Lmk_gok_6\n"
    "s_mov_b32 s78, 0\n"
    ".Lmk_gspin_7:\n"
    "s_mov_b64 exec, vcc\n"
    "buffer_load_dwordx4 v[240:243], v47, s[32:35], s20 offen sc1\n"
    "buffer_load_dwordx4 v[244:247], v47, s[32:35], s21 offen sc1\n"
    "buffer_load_dwordx4 v[248:251], v47, s[32:35], s22 offen sc1\n"
    "buffer_load_dwordx4 v[252:255], v47, s[32:35], s23 offen sc1\n"
    "s_add_u32 s78, s78, 1\n"
    "s_waitcnt vmcnt(0)\n"
    "v_xor_b32 v2, s72, v241\n"
    "v_bitop3_b32 v2, v243, v62, v2 bitop3:0xbe\n"
    "v_bitop3_b32 v2, v245, v62, v2 bitop3:0xbe\n"
    "v_bitop3_b32 v2, v247, v62, v2 bitop3:0xbe\n"
    "v_bitop3_b32 v2, v249, v62, v2 bitop3:0xbe\n"
    "v_bitop3_b32 v2, v251, v62, v2 bitop3:0xbe\n"
    "v_bitop3_b32 v2, v253, v62, v2 bitop3:0xbe\n"
    "v_bitop3_b32 v2, v255, v62, v2 bitop3:0xbe\n"
    "v_cmp_ne_u32 vcc, 0, v2\n"
    "s_nop 1\n"
    "s_cbranch_vccz .Lmk_gfin_8\n"
    "s_cmp_lt_u32 s78, 20000\n"
    "s_cbranch_scc1 .Lmk_gspin_7\n"
    ".Lmk_gfin_8:\n"
    "s_mov_b64 exec, -1\n"
    ".Lmk_gok_6:\n"
    "v_mov_b32 v241, v242\n"
    "ds_write_b64 v57, v[240:241] offset:0\n"
    "v_mov_b32 v245, v246\n"
    "ds_write_b64 v57, v[244:245] offset:2048\n"
    "v_mov_b32 v249, v250\n"
    "ds_write_b64 v57, v[248:249] offset:4096\n"
    "v_mov_b32 v253, v254\n"
    "ds_write_b64 v57, v[252:253] offset:6144\n"
    "s_waitcnt lgkmcnt(0)\n"
    "s_barrier\n"
    "ds_read_b64 v[18:19], v53 offset:0\n"
    "ds_read_b128 v[34:37], v54 offset:0\n"
    "ds_read_b64 v[20:21], v53 offset:512\n"
    "ds_read_b128 v[38:41], v54 offset:1024\n"
    "ds_read_b128 v[112:115], v52 offset:0\n"
    "ds_read_b128 v[120:123], v52 offset:1024\n"
    "s_waitcnt lgkmcnt(4)\n"
    "v_lshlrev_b32 v232, 16, v34\n"
    "v_and_b32 v34, 0xffff0000, v34\n"
    "v_add_f32 v34, s75, v34\n"
    "v_fma_mix_f32 v232, v18, v34, v232 op_sel:[0,0,0] op_sel_hi:[1,0,0]\n"
    "v_lshlrev_b32 v233, 16, v35\n"
    "v_and_b32 v35, 0xffff0000, v35\n"
    "v_add_f32 v35, s75, v35\n"
    "v_fma_mix_f32 v233, v18, v35, v233 op_sel:[1,0,0] op_sel_hi:[1,0,0]\n"
    "v_lshlrev_b32 v234, 16, v36\n"
    "v_and_b32 v36, 0xffff0000, v36\n"
    "v_add_f32 v36, s75, v36\n"
    "v_fma_mix_f32 v234, v19, v36, v234 op_sel:[0,0,0] op_sel_hi:[1,0,0]\n"
    "v_lshlrev_b32 v235, 16, v37\n"
    "v_and_b32 v37, 0xffff0000, v37\n"
    "v_add_f32 v37, s75, v37\n"
    "v_fma_mix_f32 v235, v19, v37, v235 op_sel:[1,0,0] op_sel_hi:[1,0,0]\n"
    "s_waitcnt lgkmcnt(2)\n"
    "v_lshlrev_b32 v236, 16, v38\n"
    "v_and_b32 v38, 0xffff0000, v38\n"
    "v_add_f32 v38, s75, v38\n"
    "v_fma_mix_f32 v236, v20, v38, v236 op_sel:[0,0,0] op_sel_hi:[1,0,0]\n"
    "v_lshlrev_b32 v237, 16, v39\n"
    "v_and_b32 v39, 0xffff0000, v39\n"
    "v_add_f32 v39, s75, v39\n"
    "v_fma_mix_f32 v237, v20, v39, v237 op_sel:[1,0,0] op_sel_hi:[1,0,0]\n"
    "v_lshlrev_b32 v238, 16, v40\n"
    "v_and_b32 v40, 0xffff0000, v40\n"
    "v_add_f32 v40, s75, v40\n"
    "v_fma_mix_f32 v238, v21, v40, v238 op_sel:[0,0,0] op_sel_hi:[1,0,0]\n"
    "v_lshlrev_b32 v239, 16, v41\n"
    "v_and_b32 v41, 0xffff0000, v41\n"
    "v_add_f32 v41, s75, v41\n"
    "v_fma_mix_f32 v239, v21, v41, v239 op_sel:[1,0,0] op_sel_hi:[1,0,0]\n"
    "s_waitcnt lgkmcnt(0)\n"
    "s_cmp_eq_u32 s60, 0\n"
    "s_cbranch_scc0 .Lmk_arm_9\n"
    "v_pk_add_f32 v[112:113], v[112:113], v[128:129]\n"
    "v_pk_add_f32 v[114:115], v[114:115], v[130:131]\n"
    "v_pk_add_f32 v[120:121], v[120:121], v[132:133]\n"
    "v_pk_add_f32 v[122:123], v[122:123], v[134:135]\n"
    "s_branch .Lmk_join_10\n"
    ".Lmk_arm_9:\n"
    "v_pk_add_f32 v[112:113], v[112:113], v[136:137]\n"
    "v_pk_add_f32 v[114:115], v[114:115], v[138:139]\n"
    "v_pk_add_f32 v[120:121], v[120:121], v[140:141]\n"
    "v_pk_add_f32 v[122:123], v[122:123], v[142:143]\n"
    ".Lmk_join_10:\n"
    "v_pk_fma_f32 v[112:113], v[112:113], s[76:77], v[232:233]\n"
    "v_cvt_pk_bf16_f32 v30, v112, v113\n"
    "v_pk_fma_f32 v[114:115], v[114:115], s[76:77], v[234:235]\n"
    "v_cvt_pk_bf16_f32 v31, v114, v115\n"
    "v_pk_fma_f32 v[120:121], v[120:121], s[76:77], v[236:237]\n"
    "v_cvt_pk_bf16_f32 v32, v120, v121\n"
    "v_pk_fma_f32 v[122:123], v[122:123], s[76:77], v[238:239]\n"
    "v_cvt_pk_bf16_f32 v33, v122, v123\n"
    "v_permlane16_swap_b32 v30, v32\n"
    "s_nop 0\n"
    "v_permlane16_swap_b32 v31, v33\n"
    "buffer_store_dwordx4 v[30:33], v55, s[32:35], s15 offen offset:0 sc1\n"
    "s_waitcnt vmcnt(1)\n"
    "v_cmp_ne_u64 vcc, s[72:73], v[14:15]\n"
    "s_nop 1\n"
    "s_cbranch_vccz .Lmk_ok_11\n"
    "s_mov_b32 s78, 0\n"
    ".Lmk_spin_12:\n"
    "buffer_load_dwordx2 v[14:15], v10, s[32:35], s4 offen sc1\n"
    "s_add_u32 s78, s78, 1\n"
    "s_waitcnt vmcnt(0)\n"
    "v_cmp_ne_u64 vcc, s[72:73], v[14:15]\n"
    "s_nop 1\n"
    "s_cbranch_vccz .Lmk_ok_11\n"
    "s_cmp_lt_u32 s78, 20000\n"
    "s_cbranch_scc1 .Lmk_spin_12\n"
    ".Lmk_ok_11:\n"
    "v_mfma_f32_16x16x32_f16 v[144:147], a[196:199], v[172:175], 0\n"
    "ds_read_b64 a[176:177], v49 offset:1664\n"
    "v_lshlrev_b32 v2, 1, v12\n"
    "v_mfma_f32_16x16x32_f16 v[144:147], a[200:203], v[168:171], v[144:147]\n"
    "buffer_load_dwordx4 a[0:3], v26, s[32:35], s40 offen offset:0 sc1\n"
    "v_mfma_f32_16x16x32_f16 v[148:151], a[200:203], v[172:175], 0\n"
    "ds_read_b64 a[178:179], v49 offset:1672\n"
    "v_xor_b32 v2, v2, v6\n"
    "v_mfma_f32_16x16x32_f16 v[144:147], a[204:207], v[164:167], v[144:147]\n"
    "buffer_load_dwordx4 a[4:7], v26, s[32:35], s41 offen offset:0 sc1\n"
    "v_mfma_f32_16x16x32_f16 v[148:151], a[204:207], v[168:171], v[148:151]\n"
    "ds_read_b128 a[144:147], v50 offset:2176\n"
    "v_lshlrev_b32 v2, 4, v2\n"
    "v_mfma_f32_16x16x32_f16 v[152:155], a[204:207], v[172:175], 0\n"
    "v_mfma_f32_16x16x32_f16 v[144:147], a[192:195], a[128:131], v[144:147]\n"
    "ds_read_b64 a[180:181], v49 offset:1152\n"
    "v_lshl_add_u32 v2, v7, 12, v2\n"
    "v_mfma_f32_16x16x32_f16 v[148:151], a[196:199], a[128:131], v[148:151]\n"
    "buffer_load_dwordx4 a[8:11], v26, s[32:35], s42 offen offset:0 sc1\n"
    "v_mfma_f32_16x16x32_f16 v[152:155], a[200:203], a[128:131], v[152:155]\n"
    "ds_read_b64 a[182:183], v49 offset:1160\n"
    "s_lshl3_add_u32 s68, s59, s60\n"
    "v_mfma_f32_16x16x32_f16 v[156:159], a[204:207], a[128:131], 0\n"
    "v_mfma_f32_16x16x32_f16 v[144:147], a[160:163], v[160:163], v[144:147]\n"
    "ds_read_b128 a[148:151], v50 offset:2688\n"
    "s_lshl_b32 s67, s68, 17\n"
    "v_mfma_f32_16x16x32_f16 v[148:151], a[160:163], v[164:167], v[148:151]\n"
    "buffer_load_dwordx4 a[12:15], v26, s[32:35], s43 offen offset:0 sc1\n"
    "v_mfma_f32_16x16x32_f16 v[152:155], a[160:163], v[168:171], v[152:155]\n"
    "ds_read_b64 a[184:185], v49 offset:640\n"
    "v_add_u32 v240, s67, v2\n"
    "v_mfma_f32_16x16x32_f16 v[156:159], a[160:163], v[172:175], v[156:159]\n"
    "v_mfma_f32_16x16x32_f16 v[148:151], a[192:195], a[132:135], v[148:151]\n"
    "ds_read_b64 a[186:187], v49 offset:648\n"
    "v_add_u32 v241, 31744, v240\n"
    "v_mfma_f32_16x16x32_f16 v[152:155], a[196:199], a[132:135], v[152:155]\n"
    "buffer_load_dwordx4 a[16:19], v26, s[32:35], s40 offen offset:1024 sc1\n"
    "v_mfma_f32_16x16x32_f16 v[156:159], a[200:203], a[132:135], v[156:159]\n"
    "ds_read_b128 a[152:155], v50 offset:3200\n"
    "v_add_u32 v242, 63488, v240\n"
    "v_mfma_f32_16x16x32_f16 v[148:151], a[164:167], v[160:163], v[148:151]\n"
    "v_mfma_f32_16x16x32_f16 v[152:155], a[164:167], v[164:167], v[152:155]\n"
    "ds_read_b64 a[188:189], v49 offset:128\n"
    "v_add_u32 v243, 95232, v240\n"
    "v_mfma_f32_16x16x32_f16 v[156:159], a[164:167], v[168:171], v[156:159]\n"
    "buffer_load_dwordx4 a[20:23], v26, s[32:35], s41 offen offset:1024 sc1\n"
    "v_mfma_f32_16x16x32_f16 v[152:155], a[192:195], a[136:139], v[152:155]\n"
    "ds_read_b64 a[190:191], v49 offset:136\n"
    "s_lshl_b32 s7, s61, 7\n"
    "v_mfma_f32_16x16x32_f16 v[156:159], a[196:199], a[136:139], v[156:159]\n"
    "v_mfma_f32_16x16x32_f16 v[152:155], a[168:171], v[160:163], v[152:155]\n"
    "ds_read_b128 a[156:159], v50 offset:3712\n"
    "s_add_u32 s7, s7, 16777216\n"
    "v_mfma_f32_16x16x32_f16 v[156:159], a[168:171], v[164:167], v[156:159]\n"
    "v_mfma_f32_16x16x32_f16 v[156:159], a[192:195], a[140:143], v[156:159]\n"
    "s_lshl_b32 s8, s68, 12\n"
    "s_waitcnt vmcnt(6)\n"
    "s_barrier\n"
    "s_add_u32 s67, s13, 917504\n"
    "s_mov_b64 exec, 3\n"
    "buffer_store_dwordx2 v[62:63], v44, s[32:35], s67 offen sc1\n"
    "s_mov_b64 exec, -1\n"
    "buffer_store_dwordx4 v[224:227], v24, s[36:39], s11 offen offset:0 sc1\n"
    "buffer_store_dwordx4 v[228:231], v24, s[36:39], s11 offen offset:1024 sc1\n"
    "v_mfma_f32_16x16x32_f16 v[156:159], a[172:175], v[160:163], v[156:159]\n"
    "v_mfma_f32_16x16x32_f16 v[144:147], a[212:215], v[188:191], v[144:147]\n"
    "ds_read_b64 a[160:161], v49 offset:1600\n"
    "v_mfma_f32_16x16x32_f16 v[144:147], a[216:219], v[184:187], v[144:147]\n"
    "buffer_load_dwordx4 a[24:27], v26, s[32:35], s42 offen offset:1024 sc1\n"
    "v_mfma_f32_16x16x32_f16 v[148:151], a[216:219], v[188:191], v[148:151]\n"
    "ds_read_b64 a[162:163], v49 offset:1608\n"
    "v_mfma_f32_16x16x32_f16 v[144:147], a[220:223], v[180:183], v[144:147]\n"
    "buffer_load_dwordx4 a[28:31], v26, s[32:35], s43 offen offset:1024 sc1\n"
    "v_mfma_f32_16x16x32_f16 v[148:151], a[220:223], v[184:187], v[148:151]\n"
    "ds_read_b128 a[128:131], v50 offset:2112\n"
    "v_mfma_f32_16x16x32_f16 v[152:155], a[220:223], v[188:191], v[152:155]\n"
    "s_waitcnt lgkmcnt(12)\n"
    "v_mfma_f32_16x16x32_f16 v[144:147], a[208:211], a[144:147], v[144:147]\n"
    "ds_read_b64 a[164:165], v49 offset:1088\n"
    "v_mfma_f32_16x16x32_f16 v[148:151], a[212:215], a[144:147], v[148:151]\n"
    "buffer_load_dwordx4 a[32:35], v26, s[32:35], s40 offen offset:2048 sc1\n"
    "v_mfma_f32_16x16x32_f16 v[152:155], a[216:219], a[144:147], v[152:155]\n"
    "ds_read_b64 a[166:167], v49 offset:1096\n"
    "v_mfma_f32_16x16x32_f16 v[156:159], a[220:223], a[144:147], v[156:159]\n"
    "v_mfma_f32_16x16x32_f16 v[144:147], a[176:179], v[176:179], v[144:147]\n"
    "ds_read_b128 a[132:135], v50 offset:2624\n"
    "v_mfma_f32_16x16x32_f16 v[148:151], a[176:179], v[180:183], v[148:151]\n"
    "buffer_load_dwordx4 a[36:39], v26, s[32:35], s41 offen offset:2048 sc1\n"
    "v_mfma_f32_16x16x32_f16 v[152:155], a[176:179], v[184:187], v[152:155]\n"
    "ds_read_b64 a[168:169], v49 offset:576\n"
    "v_mfma_f32_16x16x32_f16 v[156:159], a[176:179], v[188:191], v[156:159]\n"
    "buffer_load_dwordx4 a[40:43], v26, s[32:35], s42 offen offset:2048 sc1\n"
    "s_waitcnt lgkmcnt(13)\n"
    "v_mfma_f32_16x16x32_f16 v[148:151], a[208:211], a[148:151], v[148:151]\n"
    "ds_read_b64 a[170:171], v49 offset:584\n"
    "v_mfma_f32_16x16x32_f16 v[152:155], a[212:215], a[148:151], v[152:155]\n"
    "v_mfma_f32_16x16x32_f16 v[156:159], a[216:219], a[148:151], v[156:159]\n"
    "ds_read_b128 a[136:139], v50 offset:3136\n"
    "v_mfma_f32_16x16x32_f16 v[148:151], a[180:183], v[176:179], v[148:151]\n"
    "buffer_load_dwordx4 a[44:47], v26, s[32:35], s43 offen offset:2048 sc1\n"
    "v_mfma_f32_16x16x32_f16 v[152:155], a[180:183], v[180:183], v[152:155]\n"
    "ds_read_b64 a[172:173], v49 offset:64\n"
    "v_mfma_f32_16x16x32_f16 v[156:159], a[180:183], v[184:187], v[156:159]\n"
    "s_waitcnt lgkmcnt(13)\n"
    "v_mfma_f32_16x16x32_f16 v[152:155], a[208:211], a[152:155], v[152:155]\n"
    "ds_read_b64 a[174:175], v49 offset:72\n"
    "v_mfma_f32_16x16x32_f16 v[156:159], a[212:215], a[152:155], v[156:159]\n"
    "buffer_load_dwordx4 a[48:51], v26, s[32:35], s40 offen offset:3072 sc1\n"
    "v_mfma_f32_16x16x32_f16 v[152:155], a[184:187], v[176:179], v[152:155]\n"
    "ds_read_b128 a[140:143], v50 offset:3648\n"
    "v_mfma_f32_16x16x32_f16 v[156:159], a[184:187], v[180:183], v[156:159]\n"
    "s_waitcnt lgkmcnt(12)\n"
    "v_mfma_f32_16x16x32_f16 v[156:159], a[208:211], a[156:159], v[156:159]\n"
    "v_mfma_f32_16x16x32_f16 v[156:159], a[188:191], v[176:179], v[156:159]\n"
    "buffer_load_dwordx4 a[52:55], v26, s[32:35], s41 offen offset:3072 sc1\n"
    "v_mfma_f32_16x16x32_f16 v[144:147], a[228:231], v[204:207], v[144:147]\n"
    "ds_read_b64 a[176:177], v49 offset:1536\n"
    "v_mfma_f32_16x16x32_f16 v[144:147], a[232:235], v[200:203], v[144:147]\n"
    "buffer_load_dwordx4 a[56:59], v26, s[32:35], s42 offen offset:3072 sc1\n"
    "v_mfma_f32_16x16x32_f16 v[148:151], a[232:235], v[204:207], v[148:151]\n"
    "ds_read_b64 a[178:179], v49 offset:1544\n"
    "v_mfma_f32_16x16x32_f16 v[144:147], a[236:239], v[196:199], v[144:147]\n"
    "v_mfma_f32_16x16x32_f16 v[148:151], a[236:239], v[200:203], v[148:151]\n"
    "ds_read_b128 a[144:147], v50 offset:2048\n"
    "v_mfma_f32_16x16x32_f16 v[152:155], a[236:239], v[204:207], v[152:155]\n"
    "buffer_load_dwordx4 a[60:63], v26, s[32:35], s43 offen offset:3072 sc1\n"
    "s_waitcnt lgkmcnt(12)\n"
    "v_mfma_f32_16x16x32_f16 v[144:147], a[224:227], a[128:131], v[144:147]\n"
    "ds_read_b64 a[180:181], v49 offset:1024\n"
    "v_mfma_f32_16x16x32_f16 v[148:151], a[228:231], a[128:131], v[148:151]\n"
    "v_mfma_f32_16x16x32_f16 v[152:155], a[232:235], a[128:131], v[152:155]\n"
    "ds_read_b64 a[182:183], v49 offset:1032\n"
    "v_mfma_f32_16x16x32_f16 v[156:159], a[236:239], a[128:131], v[156:159]\n"
    "buffer_load_dwordx4 a[64:67], v27, s[32:35], s40 offen offset:0 sc1\n"
    "v_mfma_f32_16x16x32_f16 v[144:147], a[160:163], v[192:195], v[144:147]\n"
    "ds_read_b128 a[148:151], v50 offset:2560\n"
    "v_mfma_f32_16x16x32_f16 v[148:151], a[160:163], v[196:199], v[148:151]\n"
    "buffer_load_dwordx4 a[68:71], v27, s[32:35], s41 offen offset:0 sc1\n"
    "v_mfma_f32_16x16x32_f16 v[152:155], a[160:163], v[200:203], v[152:155]\n"
    "ds_read_b64 a[184:185], v49 offset:512\n"
    "v_mfma_f32_16x16x32_f16 v[156:159], a[160:163], v[204:207], v[156:159]\n"
    "s_waitcnt lgkmcnt(13)\n"
    "v_mfma_f32_16x16x32_f16 v[148:151], a[224:227], a[132:135], v[148:151]\n"
    "ds_read_b64 a[186:187], v49 offset:520\n"
    "v_mfma_f32_16x16x32_f16 v[152:155], a[228:231], a[132:135], v[152:155]\n"
    "buffer_load_dwordx4 a[72:75], v27, s[32:35], s42 offen offset:0 sc1\n"
    "v_mfma_f32_16x16x32_f16 v[156:159], a[232:235], a[132:135], v[156:159]\n"
    "ds_read_b128 a[152:155], v50 offset:3072\n"
    "v_mfma_f32_16x16x32_f16 v[148:151], a[164:167], v[192:195], v[148:151]\n"
    "v_mfma_f32_16x16x32_f16 v[152:155], a[164:167], v[196:199], v[152:155]\n"
    "ds_read_b64 a[188:189], v49 offset:0\n"
    "v_mfma_f32_16x16x32_f16 v[156:159], a[164:167], v[200:203], v[156:159]\n"
    "buffer_load_dwordx4 a[76:79], v27, s[32:35], s43 offen offset:0 sc1\n"
    "s_waitcnt lgkmcnt(13)\n"
    "v_mfma_f32_16x16x32_f16 v[152:155], a[224:227], a[136:139], v[152:155]\n"
    "ds_read_b64 a[190:191], v49 offset:8\n"
    "v_mfma_f32_16x16x32_f16 v[156:159], a[228:231], a[136:139], v[156:159]\n"
    "v_mfma_f32_16x16x32_f16 v[152:155], a[168:171], v[192:195], v[152:155]\n"
    "ds_read_b128 a[156:159], v50 offset:3584\n"
    "v_mfma_f32_16x16x32_f16 v[156:159], a[168:171], v[196:199], v[156:159]\n"
    "buffer_load_dwordx4 a[80:83], v27, s[32:35], s40 offen offset:1024 sc1\n"
    "s_waitcnt lgkmcnt(12)\n"
    "v_mfma_f32_16x16x32_f16 v[156:159], a[224:227], a[140:143], v[156:159]\n"
    "v_mfma_f32_16x16x32_f16 v[156:159], a[172:175], v[192:195], v[156:159]\n"
    "buffer_load_dwordx4 a[84:87], v27, s[32:35], s41 offen offset:1024 sc1\n"
    "v_mfma_f32_16x16x32_f16 v[144:147], a[244:247], v[220:223], v[144:147]\n"
    "ds_read_b64 v[18:19], v53 offset:2048\n"
    "v_mfma_f32_16x16x32_f16 v[144:147], a[248:251], v[216:219], v[144:147]\n"
    "v_mfma_f32_16x16x32_f16 v[148:151], a[248:251], v[220:223], v[148:151]\n"
    "ds_read_b128 v[34:37], v54 offset:4096\n"
    "v_mfma_f32_16x16x32_f16 v[144:147], a[252:255], v[212:215], v[144:147]\n"
    "buffer_load_dwordx4 a[88:91], v27, s[32:35], s42 offen offset:1024 sc1\n"
    "v_mfma_f32_16x16x32_f16 v[148:151], a[252:255], v[216:219], v[148:151]\n"
    "ds_read_b64 v[20:21], v53 offset:2560\n"
    "v_mfma_f32_16x16x32_f16 v[152:155], a[252:255], v[220:223], v[152:155]\n"
    "s_waitcnt lgkmcnt(12)\n"
    "v_mfma_f32_16x16x32_f16 v[144:147], a[240:243], a[144:147], v[144:147]\n"
    "ds_read_b128 v[38:41], v54 offset:5120\n"
    "v_mfma_f32_16x16x32_f16 v[148:151], a[244:247], a[144:147], v[148:151]\n"
    "buffer_load_dwordx4 a[92:95], v27, s[32:35], s43 offen offset:1024 sc1\n"
    "v_mfma_f32_16x16x32_f16 v[152:155], a[248:251], a[144:147], v[152:155]\n"
    "v_mfma_f32_16x16x32_f16 v[156:159], a[252:255], a[144:147], v[156:159]\n"
    "s_waitcnt lgkmcnt(2)\n"
    "v_lshlrev_b32 v232, 16, v34\n"
    "v_and_b32 v34, 0xffff0000, v34\n"
    "v_mfma_f32_16x16x32_f16 v[144:147], a[176:179], v[208:211], v[144:147]\n"
    "v_add_f32 v34, s75, v34\n"
    "v_fma_mix_f32 v232, v18, v34, v232 op_sel:[0,0,0] op_sel_hi:[1,0,0]\n"
    "v_mfma_f32_16x16x32_f16 v[148:151], a[176:179], v[212:215], v[148:151]\n"
    "v_lshlrev_b32 v233, 16, v35\n"
    "v_and_b32 v35, 0xffff0000, v35\n"
    "v_mfma_f32_16x16x32_f16 v[152:155], a[176:179], v[216:219], v[152:155]\n"
    "v_add_f32 v35, s75, v35\n"
    "v_fma_mix_f32 v233, v18, v35, v233 op_sel:[1,0,0] op_sel_hi:[1,0,0]\n"
    "s_add_u32 s4, s14, 917504\n"
    "buffer_load_dwordx2 v[14:15], v45, s[32:35], s4 offen sc1\n"
    "v_mfma_f32_16x16x32_f16 v[156:159], a[176:179], v[220:223], v[156:159]\n"
    "v_lshlrev_b32 v234, 16, v36\n"
    "v_and_b32 v36, 0xffff0000, v36\n"
    "v_mfma_f32_16x16x32_f16 v[148:151], a[240:243], a[148:151], v[148:151]\n"
    "v_add_f32 v36, s75, v36\n"
    "v_fma_mix_f32 v234, v19, v36, v234 op_sel:[0,0,0] op_sel_hi:[1,0,0]\n"
    "v_mfma_f32_16x16x32_f16 v[152:155], a[244:247], a[148:151], v[152:155]\n"
    "v_lshlrev_b32 v235, 16, v37\n"
    "v_and_b32 v37, 0xffff0000, v37\n"
    "v_mfma_f32_16x16x32_f16 v[156:159], a[248:251], a[148:151], v[156:159]\n"
    "v_add_f32 v37, s75, v37\n"
    "v_fma_mix_f32 v235, v19, v37, v235 op_sel:[1,0,0] op_sel_hi:[1,0,0]\n"
    "v_mfma_f32_16x16x32_f16 v[148:151], a[180:183], v[208:211], v[148:151]\n"
    "s_waitcnt lgkmcnt(0)\n"
    "v_lshlrev_b32 v236, 16, v38\n"
    "v_and_b32 v38, 0xffff0000, v38\n"
    "v_mfma_f32_16x16x32_f16 v[152:155], a[180:183], v[212:215], v[152:155]\n"
    "v_add_f32 v38, s75, v38\n"
    "v_fma_mix_f32 v236, v20, v38, v236 op_sel:[0,0,0] op_sel_hi:[1,0,0]\n"
    "v_mfma_f32_16x16x32_f16 v[156:159], a[180:183], v[216:219], v[156:159]\n"
    "v_lshlrev_b32 v237, 16, v39\n"
    "v_and_b32 v39, 0xffff0000, v39\n"
    "v_mfma_f32_16x16x32_f16 v[152:155], a[240:243], a[152:155], v[152:155]\n"
    "v_add_f32 v39, s75, v39\n"
    "v_fma_mix_f32 v237, v20, v39, v237 op_sel:[1,0,0] op_sel_hi:[1,0,0]\n"
    "v_mfma_f32_16x16x32_f16 v[156:159], a[244:247], a[152:155], v[156:159]\n"
    "v_lshlrev_b32 v238, 16, v40\n"
    "v_and_b32 v40, 0xffff0000, v40\n"
    "v_mfma_f32_16x16x32_f16 v[152:155], a[184:187], v[208:211], v[152:155]\n"
    "v_add_f32 v40, s75, v40\n"
    "v_fma_mix_f32 v238, v21, v40, v238 op_sel:[0,0,0] op_sel_hi:[1,0,0]\n"
    "v_mfma_f32_16x16x32_f16 v[156:159], a[184:187], v[212:215], v[156:159]\n"
    "v_lshlrev_b32 v239, 16, v41\n"
    "v_and_b32 v41, 0xffff0000, v41\n"
    "v_mfma_f32_16x16x32_f16 v[156:159], a[240:243], a[156:159], v[156:159]\n"
    "v_add_f32 v41, s75, v41\n"
    "v_fma_mix_f32 v239, v21, v41, v239 op_sel:[1,0,0] op_sel_hi:[1,0,0]\n"
    "v_mfma_f32_16x16x32_f16 v[156:159], a[188:191], v[208:211], v[156:159]\n"
    "s_cmp_eq_u32 s60, 0\n"
    "s_cbranch_scc0 .Lmk_arm_13\n"
    "ds_write_b128 v51, v[152:155] offset:0\n"
    "s_nop 4\n"
    "ds_write_b128 v51, v[156:159] offset:1024\n"
    "s_branch .Lmk_join_14\n"
    ".Lmk_arm_13:\n"
    "ds_write_b128 v51, v[144:147] offset:0\n"
    "ds_write_b128 v51, v[148:151] offset:1024\n"
    ".Lmk_join_14:\n"
    "s_waitcnt vmcnt(0)\n"
    "v_cmp_ne_u64 vcc, s[72:73], v[14:15]\n"
    "s_nop 1\n"
    "s_cbranch_vccz .Lmk_ok_15\n"
    "s_mov_b32 s78, 0\n"
    ".Lmk_spin_16:\n"
    "buffer_load_dwordx2 v[14:15], v45, s[32:35], s4 offen sc1\n"
    "s_add_u32 s78, s78, 1\n"
    "s_waitcnt vmcnt(0)\n"
    "v_cmp_ne_u64 vcc, s[72:73], v[14:15]\n"
    "s_nop 1\n"
    "s_cbranch_vccz .Lmk_ok_15\n"
    "s_cmp_lt_u32 s78, 20000\n"
    "s_cbranch_scc1 .Lmk_spin_16\n"
    ".Lmk_ok_15:\n"
    "s_waitcnt lgkmcnt(0)\n"
    "s_barrier\n"
    "s_add_u32 m0, s8, 0\n"
    "s_add_u32 s9, s7, 0\n"
    "buffer_load_dwordx4 v240, s[32:35], s9 offen offset:0 sc1 lds\n"
    "buffer_load_dwordx4 v241, s[32:35], s9 offen offset:1024 sc1 lds\n"
    "buffer_load_dwordx4 v242, s[32:35], s9 offen offset:2048 sc1 lds\n"
    "buffer_load_dwordx4 v243, s[32:35], s9 offen offset:3072 sc1 lds\n"
    "s_add_u32 m0, s8, 8192\n"
    "s_add_u32 s9, s7, 262144\n"
    "buffer_load_dwordx4 v240, s[32:35], s9 offen offset:0 sc1 lds\n"
    "buffer_load_dwordx4 v241, s[32:35], s9 offen offset:1024 sc1 lds\n"
    "buffer_load_dwordx4 v242, s[32:35], s9 offen offset:2048 sc1 lds\n"
    "buffer_load_dwordx4 v243, s[32:35], s9 offen offset:3072 sc1 lds\n"
    "ds_read_b128 v[112:115], v52 offset:0\n"
    "ds_read_b128 v[120:123], v52 offset:1024\n"
    "s_waitcnt lgkmcnt(0)\n"
    "s_cmp_eq_u32 s60, 0\n"
    "s_cbranch_scc0 .Lmk_arm_17\n"
    "v_pk_add_f32 v[112:113], v[112:113], v[144:145]\n"
    "v_pk_add_f32 v[114:115], v[114:115], v[146:147]\n"
    "v_pk_add_f32 v[120:121], v[120:121], v[148:149]\n"
    "v_pk_add_f32 v[122:123], v[122:123], v[150:151]\n"
    "s_branch .Lmk_join_18\n"
    ".Lmk_arm_17:\n"
    "v_pk_add_f32 v[112:113], v[112:113], v[152:153]\n"
    "v_pk_add_f32 v[114:115], v[114:115], v[154:155]\n"
    "v_pk_add_f32 v[120:121], v[120:121], v[156:157]\n"
    "v_pk_add_f32 v[122:123], v[122:123], v[158:159]\n"
    ".Lmk_join_18:\n"
    "v_pk_fma_f32 v[112:113], v[112:113], s[76:77], v[232:233]\n"
    "v_cvt_pk_bf16_f32 v30, v112, v113\n"
    "v_pk_fma_f32 v[114:115], v[114:115], s[76:77], v[234:235]\n"
    "v_cvt_pk_bf16_f32 v31, v114, v115\n"
    "v_pk_fma_f32 v[120:121], v[120:121], s[76:77], v[236:237]\n"
    "v_cvt_pk_bf16_f32 v32, v120, v121\n"
    "v_pk_fma_f32 v[122:123], v[122:123], s[76:77], v[238:239]\n"
    "v_cvt_pk_bf16_f32 v33, v122, v123\n"
    "v_permlane16_swap_b32 v30, v32\n"
    "s_nop 0\n"
    "v_permlane16_swap_b32 v31, v33\n"
    "buffer_store_dwordx4 v[30:33], v55, s[32:35], s15 offen offset:2048 sc1\n"
    "s_add_u32 m0, s8, 16384\n"
    "s_add_u32 s9, s7, 524288\n"
    "buffer_load_dwordx4 v240, s[32:35], s9 offen offset:0 sc1 lds\n"
    "buffer_load_dwordx4 v241, s[32:35], s9 offen offset:1024 sc1 lds\n"
    "buffer_load_dwordx4 v242, s[32:35], s9 offen offset:2048 sc1 lds\n"
    "buffer_load_dwordx4 v243, s[32:35], s9 offen offset:3072 sc1 lds\n"
    "s_add_u32 m0, s8, 24576\n"
    "s_add_u32 s9, s7, 786432\n"
    "buffer_load_dwordx4 v240, s[32:35], s9 offen offset:0 sc1 lds\n"
    "buffer_load_dwordx4 v241, s[32:35], s9 offen offset:1024 sc1 lds\n"
    "buffer_load_dwordx4 v242, s[32:35], s9 offen offset:2048 sc1 lds\n"
    "buffer_load_dwordx4 v243, s[32:35], s9 offen offset:3072 sc1 lds\n"
    "v_lshlrev_b32 v2, 6, v6\n"
    "v_lshl_add_u32 v5, v12, 4, v2\n"
    "s_lshl_b32 s10, s5, 9\n"
    "s_add_u32 s10, s10, 393216\n"
    "s_lshl_b32 s11, s6, 9\n"
    "s_add_u32 s11, s11, 393216\n"
    "v_bfe_u32 v3, v43, 1, 2\n"
    "v_xor_b32 v3, s56, v3\n"
    "v_lshl_add_u32 v2, v43, 7, v8\n"
    "v_lshl_add_u32 v18, v3, 5, v2\n"
    "s_lshl_b32 s67, s62, 12\n"
    "v_add_u32 v10, s67, v18\n"
    "s_lshl_b32 s48, s59, 16\n"
    "s_lshl_b32 s67, s59, 15\n"
    "v_add_u32 v18, s67, v18\n"
    "v_xor_b32 v19, 32, v18\n"
    "v_xor_b32 v20, 64, v18\n"
    "v_xor_b32 v21, 96, v18\n"
    "s_add_u32 s49, s48, 65536\n"
    "s_lshl_b32 s67, s56, 11\n"
    "s_add_u32 s67, s67, 131072\n"
    "v_add_u32 v250, s67, v24\n"
    "v_xor_b32 v251, 4096, v250\n"
    "v_xor_b32 v2, s56, v12\n"
    "v_lshl_add_u32 v2, v2, 4, v11\n"
    "v_lshlrev_b32 v13, 4, v2\n"
    "s_mul_i32 s67, s61, 224\n"
    "s_add_u32 s12, s67, s5\n"
    "s_lshl_b32 s12, s12, 10\n"
    "s_add_u32 s12, s12, 18874368\n"
    "s_add_u32 s16, s67, s6\n"
    "s_lshl_b32 s16, s16, 10\n"
    "s_add_u32 s16, s16, 18874368\n"
    "s_lshl_b32 s67, s56, 6\n"
    "v_lshl_add_u32 v25, v11, 2, s67\n"
    "v_add_u32 v23, 162048, v25\n"
    "v_lshlrev_b32 v22, 1, v25\n"
    "v_mov_b32 v46, v23\n"
    "v_xor_b32 v47, 64, v23\n"
    "v_xor_b32 v48, 128, v23\n"
    "v_xor_b32 v49, 192, v23\n"
    "s_lshl4_add_u32 s17, s61, s62\n"
    "s_lshl_b32 s17, s17, 9\n"
    "s_add_u32 s17, s17, 131072\n"
    "v_cmp_eq_u32 s[24:25], 8, v8\n"
    "v_cmp_eq_u32 s[30:31], 16, v8\n"
    "v_cmp_eq_u32 s[44:45], 24, v8\n"
    "v_cmp_eq_u32 s[46:47], v9, v12\n"
    "s_lshl4_add_u32 s18, s56, s61\n"
    "s_lshl4_add_u32 s18, s18, s62\n"
    "s_lshl_b32 s18, s18, 5\n"
    "v_mov_b32 v50, 0\n"
    "s_mul_i32 s67, s56, 458752\n"
    "s_lshl_b32 s68, s65, 12\n"
    "s_add_u32 s67, s67, s68\n"
    "s_lshl_b32 s68, s59, 11\n"
    "s_add_u32 s69, s67, s68\n"
    "v_add_u32 v51, s69, v24\n"
    "v_xor_b32 v52, 2048, v51\n"
    "v_lshlrev_b32 v2, 4, v11\n"
    "v_lshl_add_u32 v2, v12, 10, v2\n"
    "s_mul_i32 s67, s56, 57344\n"
    "s_lshl1_add_u32 s70, s64, s60\n"
    "s_lshl_b32 s68, s70, 8\n"
    "s_add_u32 s69, s67, s68\n"
    "v_add_u32 v53, s69, v2\n"
    "v_xor_b32 v54, 256, v53\n"
    "s_mul_i32 s19, s63, 229376\n"
    "s_add_u32 s19, s19, 18874368\n"
    "v_lshlrev_b32 v55, 5, v11\n"
    "s_lshl4_add_u32 s20, s56, s63\n"
    "s_lshl_b32 s20, s20, 9\n"
    "v_lshlrev_b32 v2, 12, v43\n"
    "v_lshl_add_u32 v2, v8, 1, v2\n"
    "s_lshl2_add_u32 s67, s65, s56\n"
    "s_lshl_b32 s67, s67, 16\n"
    "s_lshl1_add_u32 s68, s63, s64\n"
    "s_lshl_b32 s68, s68, 6\n"
    "s_add_u32 s67, s67, s68\n"
    "v_add_u32 v56, s67, v2\n"
    "v_lshl_add_u32 v2, v43, 6, v8\n"
    "s_lshl_b32 s67, s59, 11\n"
    "s_lshl_b32 s68, s60, 5\n"
    "s_add_u32 s67, s67, s68\n"
    "s_add_u32 s67, s67, 147456\n"
    "v_add_u32 v57, s67, v2\n"
    "s_lshl_b32 s67, s70, 6\n"
    "v_lshl_add_u32 v2, v11, 2, s67\n"
    "v_lshlrev_b32 v2, 1, v2\n"
    "v_lshl_add_u32 v244, v12, 11, v2\n"
    "s_lshl_b32 s23, s63, 13\n"
    "s_add_u32 s23, s23, 131072\n"
    "s_lshl2_add_u32 s67, s63, s70\n"
    "s_lshl_b32 s67, s67, 14\n"
    "s_lshl1_add_u32 s68, s65, s59\n"
    "s_lshl_b32 s68, s68, 6\n"
    "s_add_u32 s67, s67, s68\n"
    "v_lshl_add_u32 v2, v11, 10, s67\n"
    "v_and_b32 v3, 1, v12\n"
    "v_lshl_add_u32 v2, v3, 5, v2\n"
    "v_lshrrev_b32 v3, 1, v12\n"
    "v_lshl_add_u32 v245, v3, 4, v2\n"
    "s_xor_b32 s67, s56, 1\n"
    "s_mul_i32 s67, s67, 6144\n"
    "s_add_u32 s67, s67, 0\n"
    "v_add_u32 v246, s67, v24\n"
    "s_xor_b32 s67, s56, 2\n"
    "s_mul_i32 s67, s67, 6144\n"
    "s_add_u32 s67, s67, 2048\n"
    "v_add_u32 v247, s67, v24\n"
    "s_xor_b32 s67, s56, 3\n"
    "s_mul_i32 s67, s67, 6144\n"
    "s_add_u32 s67, s67, 4096\n"
    "v_add_u32 v248, s67, v24\n"
    "s_mul_i32 s67, s56, 6144\n"
    "v_add_u32 v249, s67, v24\n"
    "s_waitcnt vmcnt(9)\n"
    "s_waitcnt vmcnt(8)\n"
    "s_barrier\n"
    "ds_read_b64_tr_b16 v[96:97], v18 offset:0\n"
    "ds_read_b64_tr_b16 v[98:99], v18 offset:2048\n"
    "ds_read_b64_tr_b16 v[100:101], v19 offset:0\n"
    "ds_read_b64_tr_b16 v[102:103], v19 offset:2048\n"
    "ds_read_b64_tr_b16 v[104:105], v20 offset:0\n"
    "ds_read_b64_tr_b16 v[106:107], v20 offset:2048\n"
    "ds_read_b64_tr_b16 v[108:109], v21 offset:0\n"
    "ds_read_b64_tr_b16 v[110:111], v21 offset:2048\n"
    "s_add_u32 s67, s13, 1179648\n"
    "s_mov_b64 exec, 3\n"
    "buffer_store_dwordx2 v[62:63], v44, s[32:35], s67 offen sc1\n"
    "s_mov_b64 exec, -1\n"
    "s_waitcnt lgkmcnt(6)\n"
    "v_mfma_f32_16x16x32_bf16 v[6:9], v[58:61], v[96:99], 0\n"
    "ds_read_b64_tr_b16 v[80:81], v18 offset:4096\n"
    "v_mfma_f32_16x16x32_bf16 v[168:171], a[0:3], v[96:99], 0\n"
    "ds_read_b64_tr_b16 v[82:83], v18 offset:6144\n"
    "v_mfma_f32_16x16x32_bf16 v[184:187], a[4:7], v[96:99], 0\n"
    "ds_read_b64_tr_b16 v[84:85], v19 offset:4096\n"
    "v_mfma_f32_16x16x32_bf16 v[200:203], a[8:11], v[96:99], 0\n"
    "ds_read_b64_tr_b16 v[86:87], v19 offset:6144\n"
    "v_mfma_f32_16x16x32_bf16 v[216:219], a[12:15], v[96:99], 0\n"
    "ds_read_b64_tr_b16 v[88:89], v20 offset:4096\n"
    "s_waitcnt lgkmcnt(9)\n"
    "v_mfma_f32_16x16x32_bf16 v[172:175], a[0:3], v[100:103], 0\n"
    "ds_read_b64_tr_b16 v[90:91], v20 offset:6144\n"
    "v_mfma_f32_16x16x32_bf16 v[188:191], a[4:7], v[100:103], 0\n"
    "ds_read_b64_tr_b16 v[92:93], v21 offset:4096\n"
    "v_mfma_f32_16x16x32_bf16 v[204:207], a[8:11], v[100:103], 0\n"
    "ds_read_b64_tr_b16 v[94:95], v21 offset:6144\n"
    "v_mfma_f32_16x16x32_bf16 v[220:223], a[12:15], v[100:103], 0\n"
    "buffer_load_dwordx4 a[96:99], v27, s[32:35], s40 offen offset:2048 sc1\n"
    "s_waitcnt lgkmcnt(10)\n"
    "v_mfma_f32_16x16x32_bf16 v[176:179], a[0:3], v[104:107], 0\n"
    "v_mfma_f32_16x16x32_bf16 v[192:195], a[4:7], v[104:107], 0\n"
    "buffer_load_dwordx4 a[100:103], v27, s[32:35], s41 offen offset:2048 sc1\n"
    "v_mfma_f32_16x16x32_bf16 v[208:211], a[8:11], v[104:107], 0\n"
    "v_mfma_f32_16x16x32_bf16 v[72:75], a[12:15], v[104:107], 0\n"
    "buffer_load_dwordx4 a[104:107], v27, s[32:35], s42 offen offset:2048 sc1\n"
    "s_waitcnt lgkmcnt(8)\n"
    "v_mfma_f32_16x16x32_bf16 v[180:183], a[0:3], v[108:111], 0\n"
    "v_mfma_f32_16x16x32_bf16 v[196:199], a[4:7], v[108:111], 0\n"
    "buffer_load_dwordx4 a[108:111], v27, s[32:35], s43 offen offset:2048 sc1\n"
    "v_mfma_f32_16x16x32_bf16 v[212:215], a[8:11], v[108:111], 0\n"
    "v_mfma_f32_16x16x32_bf16 v[76:79], a[12:15], v[108:111], 0\n"
    "s_waitcnt lgkmcnt(6)\n"
    "v_mfma_f32_16x16x32_bf16 v[6:9], v[58:61], v[80:83], v[6:9]\n"
    "ds_read_b64_tr_b16 v[96:97], v18 offset:8192\n"
    "v_mfma_f32_16x16x32_bf16 v[168:171], a[16:19], v[80:83], v[168:171]\n"
    "ds_read_b64_tr_b16 v[98:99], v18 offset:10240\n"
    "v_mfma_f32_16x16x32_bf16 v[184:187], a[20:23], v[80:83], v[184:187]\n"
    "ds_read_b64_tr_b16 v[100:101], v19 offset:8192\n"
    "v_mfma_f32_16x16x32_bf16 v[200:203], a[24:27], v[80:83], v[200:203]\n"
    "ds_read_b64_tr_b16 v[102:103], v19 offset:10240\n"
    "v_mfma_f32_16x16x32_bf16 v[216:219], a[28:31], v[80:83], v[216:219]\n"
    "ds_read_b64_tr_b16 v[104:105], v20 offset:8192\n"
    "s_waitcnt lgkmcnt(9)\n"
    "v_mfma_f32_16x16x32_bf16 v[172:175], a[16:19], v[84:87], v[172:175]\n"
    "ds_read_b64_tr_b16 v[106:107], v20 offset:10240\n"
    "v_mfma_f32_16x16x32_bf16 v[188:191], a[20:23], v[84:87], v[188:191]\n"
    "ds_read_b64_tr_b16 v[108:109], v21 offset:8192\n"
    "v_mfma_f32_16x16x32_bf16 v[204:207], a[24:27], v[84:87], v[204:207]\n"
    "ds_read_b64_tr_b16 v[110:111], v21 offset:10240\n"
    "v_mfma_f32_16x16x32_bf16 v[220:223], a[28:31], v[84:87], v[220:223]\n"
    "buffer_load_dwordx4 a[112:115], v27, s[32:35], s40 offen offset:3072 sc1\n"
    "s_waitcnt lgkmcnt(10)\n"
    "v_mfma_f32_16x16x32_bf16 v[176:179], a[16:19], v[88:91], v[176:179]\n"
    "v_mfma_f32_16x16x32_bf16 v[192:195], a[20:23], v[88:91], v[192:195]\n"
    "buffer_load_dwordx4 a[116:119], v27, s[32:35], s41 offen offset:3072 sc1\n"
    "v_mfma_f32_16x16x32_bf16 v[208:211], a[24:27], v[88:91], v[208:211]\n"
    "v_mfma_f32_16x16x32_bf16 v[72:75], a[28:31], v[88:91], v[72:75]\n"
    "buffer_load_dwordx4 a[120:123], v27, s[32:35], s42 offen offset:3072 sc1\n"
    "s_waitcnt lgkmcnt(8)\n"
    "v_mfma_f32_16x16x32_bf16 v[180:183], a[16:19], v[92:95], v[180:183]\n"
    "v_mfma_f32_16x16x32_bf16 v[196:199], a[20:23], v[92:95], v[196:199]\n"
    "buffer_load_dwordx4 a[124:127], v27, s[32:35], s43 offen offset:3072 sc1\n"
    "v_mfma_f32_16x16x32_bf16 v[212:215], a[24:27], v[92:95], v[212:215]\n"
    "v_mfma_f32_16x16x32_bf16 v[76:79], a[28:31], v[92:95], v[76:79]\n"
    "s_waitcnt lgkmcnt(6)\n"
    "v_mfma_f32_16x16x32_bf16 v[6:9], v[58:61], v[96:99], v[6:9]\n"
    "ds_read_b64_tr_b16 v[80:81], v18 offset:12288\n"
    "v_mfma_f32_16x16x32_bf16 v[168:171], a[32:35], v[96:99], v[168:171]\n"
    "ds_read_b64_tr_b16 v[82:83], v18 offset:14336\n"
    "v_mfma_f32_16x16x32_bf16 v[184:187], a[36:39], v[96:99], v[184:187]\n"
    "ds_read_b64_tr_b16 v[84:85], v19 offset:12288\n"
    "v_mfma_f32_16x16x32_bf16 v[200:203], a[40:43], v[96:99], v[200:203]\n"
    "ds_read_b64_tr_b16 v[86:87], v19 offset:14336\n"
    "v_mfma_f32_16x16x32_bf16 v[216:219], a[44:47], v[96:99], v[216:219]\n"
    "ds_read_b64_tr_b16 v[88:89], v20 offset:12288\n"
    "s_waitcnt lgkmcnt(9)\n"
    "v_mfma_f32_16x16x32_bf16 v[172:175], a[32:35], v[100:103], v[172:175]\n"
    "ds_read_b64_tr_b16 v[90:91], v20 offset:14336\n"
    "v_mfma_f32_16x16x32_bf16 v[188:191], a[36:39], v[100:103], v[188:191]\n"
    "ds_read_b64_tr_b16 v[92:93], v21 offset:12288\n"
    "v_mfma_f32_16x16x32_bf16 v[204:207], a[40:43], v[100:103], v[204:207]\n"
    "ds_read_b64_tr_b16 v[94:95], v21 offset:14336\n"
    "v_mfma_f32_16x16x32_bf16 v[220:223], a[44:47], v[100:103], v[220:223]\n"
    "buffer_load_dwordx4 v[224:227], v5, s[32:35], s10 offen offset:0 sc1\n"
    "s_waitcnt lgkmcnt(10)\n"
    "v_mfma_f32_16x16x32_bf16 v[176:179], a[32:35], v[104:107], v[176:179]\n"
    "v_mfma_f32_16x16x32_bf16 v[192:195], a[36:39], v[104:107], v[192:195]\n"
    "buffer_load_dwordx4 v[228:231], v5, s[32:35], s10 offen offset:512 sc1\n"
    "v_mfma_f32_16x16x32_bf16 v[208:211], a[40:43], v[104:107], v[208:211]\n"
    "v_mfma_f32_16x16x32_bf16 v[72:75], a[44:47], v[104:107], v[72:75]\n"
    "buffer_load_dwordx4 v[232:235], v5, s[32:35], s10 offen offset:1024 sc1\n"
    "s_waitcnt lgkmcnt(8)\n"
    "v_mfma_f32_16x16x32_bf16 v[180:183], a[32:35], v[108:111], v[180:183]\n"
    "v_mfma_f32_16x16x32_bf16 v[196:199], a[36:39], v[108:111], v[196:199]\n"
    "buffer_load_dwordx4 v[236:239], v5, s[32:35], s11 offen sc1\n"
    "v_mfma_f32_16x16x32_bf16 v[212:215], a[40:43], v[108:111], v[212:215]\n"
    "v_mfma_f32_16x16x32_bf16 v[76:79], a[44:47], v[108:111], v[76:79]\n"
    "s_waitcnt vmcnt(13)\n"
    "s_barrier\n"
    "s_waitcnt lgkmcnt(6)\n"
    "v_mfma_f32_16x16x32_bf16 v[6:9], v[58:61], v[80:83], v[6:9]\n"
    "ds_read_b64_tr_b16 v[96:97], v18 offset:16384\n"
    "v_mfma_f32_16x16x32_bf16 v[168:171], a[48:51], v[80:83], v[168:171]\n"
    "ds_read_b64_tr_b16 v[98:99], v18 offset:18432\n"
    "v_mfma_f32_16x16x32_bf16 v[184:187], a[52:55], v[80:83], v[184:187]\n"
    "ds_read_b64_tr_b16 v[100:101], v19 offset:16384\n"
    "v_mfma_f32_16x16x32_bf16 v[200:203], a[56:59], v[80:83], v[200:203]\n"
    "ds_read_b64_tr_b16 v[102:103], v19 offset:18432\n"
    "v_mfma_f32_16x16x32_bf16 v[216:219], a[60:63], v[80:83], v[216:219]\n"
    "ds_read_b64_tr_b16 v[104:105], v20 offset:16384\n"
    "s_waitcnt lgkmcnt(9)\n"
    "v_mfma_f32_16x16x32_bf16 v[172:175], a[48:51], v[84:87], v[172:175]\n"
    "ds_read_b64_tr_b16 v[106:107], v20 offset:18432\n"
    "v_mfma_f32_16x16x32_bf16 v[188:191], a[52:55], v[84:87], v[188:191]\n"
    "ds_read_b64_tr_b16 v[108:109], v21 offset:16384\n"
    "v_mfma_f32_16x16x32_bf16 v[204:207], a[56:59], v[84:87], v[204:207]\n"
    "ds_read_b64_tr_b16 v[110:111], v21 offset:18432\n"
    "v_mfma_f32_16x16x32_bf16 v[220:223], a[60:63], v[84:87], v[220:223]\n"
    "buffer_load_dwordx4 a[128:131], v28, s[32:35], s40 offen offset:0 sc1\n"
    "s_waitcnt lgkmcnt(10)\n"
    "v_mfma_f32_16x16x32_bf16 v[176:179], a[48:51], v[88:91], v[176:179]\n"
    "v_mfma_f32_16x16x32_bf16 v[192:195], a[52:55], v[88:91], v[192:195]\n"
    "buffer_load_dwordx4 a[132:135], v28, s[32:35], s41 offen offset:0 sc1\n"
    "v_mfma_f32_16x16x32_bf16 v[208:211], a[56:59], v[88:91], v[208:211]\n"
    "v_mfma_f32_16x16x32_bf16 v[72:75], a[60:63], v[88:91], v[72:75]\n"
    "buffer_load_dwordx4 a[136:139], v28, s[32:35], s42 offen offset:0 sc1\n"
    "s_waitcnt lgkmcnt(8)\n"
    "v_mfma_f32_16x16x32_bf16 v[180:183], a[48:51], v[92:95], v[180:183]\n"
    "v_mfma_f32_16x16x32_bf16 v[196:199], a[52:55], v[92:95], v[196:199]\n"
    "buffer_load_dwordx4 a[144:147], v28, s[32:35], s40 offen offset:1024 sc1\n"
    "v_mfma_f32_16x16x32_bf16 v[212:215], a[56:59], v[92:95], v[212:215]\n"
    "v_mfma_f32_16x16x32_bf16 v[76:79], a[60:63], v[92:95], v[76:79]\n"
    "s_waitcnt lgkmcnt(6)\n"
    "v_mfma_f32_16x16x32_bf16 v[6:9], v[58:61], v[96:99], v[6:9]\n"
    "ds_read_b64_tr_b16 v[80:81], v18 offset:20480\n"
    "v_mfma_f32_16x16x32_bf16 v[168:171], a[64:67], v[96:99], v[168:171]\n"
    "ds_read_b64_tr_b16 v[82:83], v18 offset:22528\n"
    "v_mfma_f32_16x16x32_bf16 v[184:187], a[68:71], v[96:99], v[184:187]\n"
    "ds_read_b64_tr_b16 v[84:85], v19 offset:20480\n"
    "v_mfma_f32_16x16x32_bf16 v[200:203], a[72:75], v[96:99], v[200:203]\n"
    "ds_read_b64_tr_b16 v[86:87], v19 offset:22528\n"
    "v_mfma_f32_16x16x32_bf16 v[216:219], a[76:79], v[96:99], v[216:219]\n"
    "ds_read_b64_tr_b16 v[88:89], v20 offset:20480\n"
    "s_waitcnt lgkmcnt(9)\n"
    "v_mfma_f32_16x16x32_bf16 v[172:175], a[64:67], v[100:103], v[172:175]\n"
    "ds_read_b64_tr_b16 v[90:91], v20 offset:22528\n"
    "v_mfma_f32_16x16x32_bf16 v[188:191], a[68:71], v[100:103], v[188:191]\n"
    "ds_read_b64_tr_b16 v[92:93], v21 offset:20480\n"
    "v_mfma_f32_16x16x32_bf16 v[204:207], a[72:75], v[100:103], v[204:207]\n"
    "ds_read_b64_tr_b16 v[94:95], v21 offset:22528\n"
    "v_mfma_f32_16x16x32_bf16 v[220:223], a[76:79], v[100:103], v[220:223]\n"
    "buffer_load_dwordx4 a[148:151], v28, s[32:35], s41 offen offset:1024 sc1\n"
    "s_waitcnt lgkmcnt(10)\n"
    "v_mfma_f32_16x16x32_bf16 v[176:179], a[64:67], v[104:107], v[176:179]\n"
    "v_mfma_f32_16x16x32_bf16 v[192:195], a[68:71], v[104:107], v[192:195]\n"
    "buffer_load_dwordx4 a[152:155], v28, s[32:35], s42 offen offset:1024 sc1\n"
    "v_mfma_f32_16x16x32_bf16 v[208:211], a[72:75], v[104:107], v[208:211]\n"
    "v_mfma_f32_16x16x32_bf16 v[72:75], a[76:79], v[104:107], v[72:75]\n"
    "buffer_load_dwordx4 a[160:163], v28, s[32:35], s40 offen offset:2048 sc1\n"
    "s_waitcnt lgkmcnt(8)\n"
    "v_mfma_f32_16x16x32_bf16 v[180:183], a[64:67], v[108:111], v[180:183]\n"
    "v_mfma_f32_16x16x32_bf16 v[196:199], a[68:71], v[108:111], v[196:199]\n"
    "buffer_load_dwordx4 a[164:167], v28, s[32:35], s41 offen offset:2048 sc1\n"
    "v_mfma_f32_16x16x32_bf16 v[212:215], a[72:75], v[108:111], v[212:215]\n"
    "v_mfma_f32_16x16x32_bf16 v[76:79], a[76:79], v[108:111], v[76:79]\n"
    "s_add_u32 s4, s14, 1179648\n"
    "buffer_load_dwordx2 v[14:15], v45, s[32:35], s4 offen sc1\n"
    "s_waitcnt lgkmcnt(6)\n"
    "v_mfma_f32_16x16x32_bf16 v[6:9], v[58:61], v[80:83], v[6:9]\n"
    "ds_read_b64_tr_b16 v[96:97], v18 offset:24576\n"
    "v_mfma_f32_16x16x32_bf16 v[168:171], a[80:83], v[80:83], v[168:171]\n"
    "ds_read_b64_tr_b16 v[98:99], v18 offset:26624\n"
    "v_mfma_f32_16x16x32_bf16 v[184:187], a[84:87], v[80:83], v[184:187]\n"
    "ds_read_b64_tr_b16 v[100:101], v19 offset:24576\n"
    "v_mfma_f32_16x16x32_bf16 v[200:203], a[88:91], v[80:83], v[200:203]\n"
    "ds_read_b64_tr_b16 v[102:103], v19 offset:26624\n"
    "v_mfma_f32_16x16x32_bf16 v[216:219], a[92:95], v[80:83], v[216:219]\n"
    "ds_read_b64_tr_b16 v[104:105], v20 offset:24576\n"
    "s_waitcnt lgkmcnt(9)\n"
    "v_mfma_f32_16x16x32_bf16 v[172:175], a[80:83], v[84:87], v[172:175]\n"
    "ds_read_b64_tr_b16 v[106:107], v20 offset:26624\n"
    "v_mfma_f32_16x16x32_bf16 v[188:191], a[84:87], v[84:87], v[188:191]\n"
    "ds_read_b64_tr_b16 v[108:109], v21 offset:24576\n"
    "v_mfma_f32_16x16x32_bf16 v[204:207], a[88:91], v[84:87], v[204:207]\n"
    "ds_read_b64_tr_b16 v[110:111], v21 offset:26624\n"
    "v_mfma_f32_16x16x32_bf16 v[220:223], a[92:95], v[84:87], v[220:223]\n"
    "buffer_load_dwordx4 a[168:171], v28, s[32:35], s42 offen offset:2048 sc1\n"
    "s_waitcnt lgkmcnt(10)\n"
    "v_mfma_f32_16x16x32_bf16 v[176:179], a[80:83], v[88:91], v[176:179]\n"
    "v_mfma_f32_16x16x32_bf16 v[192:195], a[84:87], v[88:91], v[192:195]\n"
    "buffer_load_dwordx4 a[176:179], v28, s[32:35], s40 offen offset:3072 sc1\n"
    "v_mfma_f32_16x16x32_bf16 v[208:211], a[88:91], v[88:91], v[208:211]\n"
    "v_mfma_f32_16x16x32_bf16 v[72:75], a[92:95], v[88:91], v[72:75]\n"
    "buffer_load_dwordx4 a[180:183], v28, s[32:35], s41 offen offset:3072 sc1\n"
    "s_waitcnt lgkmcnt(8)\n"
    "v_mfma_f32_16x16x32_bf16 v[180:183], a[80:83], v[92:95], v[180:183]\n"
    "v_mfma_f32_16x16x32_bf16 v[196:199], a[84:87], v[92:95], v[196:199]\n"
    "buffer_load_dwordx4 a[184:187], v28, s[32:35], s42 offen offset:3072 sc1\n"
    "v_mfma_f32_16x16x32_bf16 v[212:215], a[88:91], v[92:95], v[212:215]\n"
    "v_mfma_f32_16x16x32_bf16 v[76:79], a[92:95], v[92:95], v[76:79]\n"
    "s_waitcnt lgkmcnt(6)\n"
    "v_mfma_f32_16x16x32_bf16 v[6:9], v[58:61], v[96:99], v[6:9]\n"
    "ds_read_b64_tr_b16 v[80:81], v18 offset:28672\n"
    "s_waitcnt vmcnt(24)\n"
    "v_mfma_f32_16x16x32_bf16 v[168:171], a[96:99], v[96:99], v[168:171]\n"
    "ds_read_b64_tr_b16 v[82:83], v18 offset:30720\n"
    "s_waitcnt vmcnt(23)\n"
    "v_mfma_f32_16x16x32_bf16 v[184:187], a[100:103], v[96:99], v[184:187]\n"
    "ds_read_b64_tr_b16 v[84:85], v19 offset:28672\n"
    "s_waitcnt vmcnt(22)\n"
    "v_mfma_f32_16x16x32_bf16 v[200:203], a[104:107], v[96:99], v[200:203]\n"
    "ds_read_b64_tr_b16 v[86:87], v19 offset:30720\n"
    "s_waitcnt vmcnt(21)\n"
    "v_mfma_f32_16x16x32_bf16 v[216:219], a[108:111], v[96:99], v[216:219]\n"
    "ds_read_b64_tr_b16 v[88:89], v20 offset:28672\n"
    "s_waitcnt lgkmcnt(9)\n"
    "v_mfma_f32_16x16x32_bf16 v[172:175], a[96:99], v[100:103], v[172:175]\n"
    "ds_read_b64_tr_b16 v[90:91], v20 offset:30720\n"
    "v_mfma_f32_16x16x32_bf16 v[188:191], a[100:103], v[100:103], v[188:191]\n"
    "ds_read_b64_tr_b16 v[92:93], v21 offset:28672\n"
    "v_mfma_f32_16x16x32_bf16 v[204:207], a[104:107], v[100:103], v[204:207]\n"
    "ds_read_b64_tr_b16 v[94:95], v21 offset:30720\n"
    "v_mfma_f32_16x16x32_bf16 v[220:223], a[108:111], v[100:103], v[220:223]\n"
    "buffer_load_dwordx4 a[192:195], v29, s[32:35], s40 offen offset:0 sc1\n"
    "s_waitcnt lgkmcnt(10)\n"
    "v_mfma_f32_16x16x32_bf16 v[176:179], a[96:99], v[104:107], v[176:179]\n"
    "ds_read_b64_tr_b16 v[30:31], v10\n"
    "v_mfma_f32_16x16x32_bf16 v[192:195], a[100:103], v[104:107], v[192:195]\n"
    "buffer_load_dwordx4 a[196:199], v29, s[32:35], s41 offen offset:0 sc1\n"
    "v_mfma_f32_16x16x32_bf16 v[208:211], a[104:107], v[104:107], v[208:211]\n"
    "ds_read_b64_tr_b16 v[32:33], v10 offset:2048\n"
    "v_mfma_f32_16x16x32_bf16 v[72:75], a[108:111], v[104:107], v[72:75]\n"
    "buffer_load_dwordx4 a[200:203], v29, s[32:35], s42 offen offset:0 sc1\n"
    "s_waitcnt lgkmcnt(10)\n"
    "v_mfma_f32_16x16x32_bf16 v[180:183], a[96:99], v[108:111], v[180:183]\n"
    "v_mfma_f32_16x16x32_bf16 v[196:199], a[100:103], v[108:111], v[196:199]\n"
    "buffer_load_dwordx4 a[208:211], v29, s[32:35], s40 offen offset:1024 sc1\n"
    "v_mfma_f32_16x16x32_bf16 v[212:215], a[104:107], v[108:111], v[212:215]\n"
    "v_mfma_f32_16x16x32_bf16 v[76:79], a[108:111], v[108:111], v[76:79]\n"
    "v_subrev_u32 v18, s48, v18\n"
    "v_subrev_u32 v19, s48, v19\n"
    "v_subrev_u32 v20, s48, v20\n"
    "v_subrev_u32 v21, s48, v21\n"
    "s_waitcnt lgkmcnt(8)\n"
    "v_mfma_f32_16x16x32_bf16 v[6:9], v[58:61], v[80:83], v[6:9]\n"
    "ds_read_b64_tr_b16 v[96:97], v18 offset:32768\n"
    "s_waitcnt vmcnt(24)\n"
    "v_mfma_f32_16x16x32_bf16 v[168:171], a[112:115], v[80:83], v[168:171]\n"
    "ds_read_b64_tr_b16 v[98:99], v18 offset:34816\n"
    "s_waitcnt vmcnt(23)\n"
    "v_mfma_f32_16x16x32_bf16 v[184:187], a[116:119], v[80:83], v[184:187]\n"
    "ds_read_b64_tr_b16 v[100:101], v19 offset:32768\n"
    "s_waitcnt vmcnt(22)\n"
    "v_mfma_f32_16x16x32_bf16 v[200:203], a[120:123], v[80:83], v[200:203]\n"
    "ds_read_b64_tr_b16 v[102:103], v19 offset:34816\n"
    "s_waitcnt vmcnt(21)\n"
    "v_mfma_f32_16x16x32_bf16 v[216:219], a[124:127], v[80:83], v[216:219]\n"
    "ds_read_b64_tr_b16 v[104:105], v20 offset:32768\n"
    "s_waitcnt lgkmcnt(11)\n"
    "v_mfma_f32_16x16x32_bf16 v[172:175], a[112:115], v[84:87], v[172:175]\n"
    "ds_read_b64_tr_b16 v[106:107], v20 offset:34816\n"
    "v_mfma_f32_16x16x32_bf16 v[188:191], a[116:119], v[84:87], v[188:191]\n"
    "ds_read_b64_tr_b16 v[108:109], v21 offset:32768\n"
    "v_mfma_f32_16x16x32_bf16 v[204:207], a[120:123], v[84:87], v[204:207]\n"
    "ds_read_b64_tr_b16 v[110:111], v21 offset:34816\n"
    "v_mfma_f32_16x16x32_bf16 v[220:223], a[124:127], v[84:87], v[220:223]\n"
    "buffer_load_dwordx4 a[212:215], v29, s[32:35], s41 offen offset:1024 sc1\n"
    "s_waitcnt lgkmcnt(12)\n"
    "v_mfma_f32_16x16x32_bf16 v[176:179], a[112:115], v[88:91], v[176:179]\n"
    "v_mfma_f32_16x16x32_bf16 v[192:195], a[116:119], v[88:91], v[192:195]\n"
    "buffer_load_dwordx4 a[216:219], v29, s[32:35], s42 offen offset:1024 sc1\n"
    "v_mfma_f32_16x16x32_bf16 v[208:211], a[120:123], v[88:91], v[208:211]\n"
    "v_mfma_f32_16x16x32_bf16 v[72:75], a[124:127], v[88:91], v[72:75]\n"
    "buffer_load_dwordx4 a[224:227], v29, s[32:35], s40 offen offset:2048 sc1\n"
    "s_waitcnt lgkmcnt(10)\n"
    "v_mfma_f32_16x16x32_bf16 v[180:183], a[112:115], v[92:95], v[180:183]\n"
    "v_mfma_f32_16x16x32_bf16 v[196:199], a[116:119], v[92:95], v[196:199]\n"
    "buffer_load_dwordx4 a[228:231], v29, s[32:35], s41 offen offset:2048 sc1\n"
    "v_mfma_f32_16x16x32_bf16 v[212:215], a[120:123], v[92:95], v[212:215]\n"
    "v_mfma_f32_16x16x32_bf16 v[76:79], a[124:127], v[92:95], v[76:79]\n"
    "s_waitcnt lgkmcnt(8)\n"
    "v_mfma_f32_16x16x32_bf16 v[252:255], v[30:33], v[30:33], 0\n"
    "s_waitcnt vmcnt(12)\n"
    "v_cmp_ne_u64 vcc, s[72:73], v[14:15]\n"
    "s_nop 1\n"
    "s_cbranch_vccz .Lmk_ok_19\n"
    "s_mov_b32 s78, 0\n"
    ".Lmk_spin_20:\n"
    "buffer_load_dwordx2 v[14:15], v45, s[32:35], s4 offen sc1\n"
    "s_add_u32 s78, s78, 1\n"
    "s_waitcnt vmcnt(0)\n"
    "v_cmp_ne_u64 vcc, s[72:73], v[14:15]\n"
    "s_nop 1\n"
    "s_cbranch_vccz .Lmk_ok_19\n"
    "s_cmp_lt_u32 s78, 20000\n"
    "s_cbranch_scc1 .Lmk_spin_20\n"
    ".Lmk_ok_19:\n"
    "s_barrier\n"
    "s_waitcnt lgkmcnt(6)\n"
    "v_mfma_f32_16x16x32_bf16 v[6:9], v[58:61], v[96:99], v[6:9]\n"
    "ds_read_b64_tr_b16 v[80:81], v18 offset:36864\n"
    "v_add_f32_dpp v224, v224, v224 row_ror:4 row_mask:0xf bank_mask:0xf\n"
    "v_mfma_f32_16x16x32_bf16 v[168:171], a[128:131], v[96:99], v[168:171]\n"
    "ds_read_b64_tr_b16 v[82:83], v18 offset:38912\n"
    "v_add_f32_dpp v225, v225, v225 row_ror:4 row_mask:0xf bank_mask:0xf\n"
    "v_mfma_f32_16x16x32_bf16 v[184:187], a[132:135], v[96:99], v[184:187]\n"
    "ds_read_b64_tr_b16 v[84:85], v19 offset:36864\n"
    "v_add_f32_dpp v226, v226, v226 row_ror:4 row_mask:0xf bank_mask:0xf\n"
    "v_mfma_f32_16x16x32_bf16 v[200:203], a[136:139], v[96:99], v[200:203]\n"
    "ds_read_b64_tr_b16 v[86:87], v19 offset:38912\n"
    "v_add_f32_dpp v227, v227, v227 row_ror:4 row_mask:0xf bank_mask:0xf\n"
    "s_waitcnt lgkmcnt(8)\n"
    "v_mfma_f32_16x16x32_bf16 v[172:175], a[128:131], v[100:103], v[172:175]\n"
    "ds_read_b64_tr_b16 v[88:89], v20 offset:36864\n"
    "v_add_f32_dpp v228, v228, v228 row_ror:4 row_mask:0xf bank_mask:0xf\n"
    "v_mfma_f32_16x16x32_bf16 v[188:191], a[132:135], v[100:103], v[188:191]\n"
    "ds_read_b64_tr_b16 v[90:91], v20 offset:38912\n"
    "v_add_f32_dpp v229, v229, v229 row_ror:4 row_mask:0xf bank_mask:0xf\n"
    "v_mfma_f32_16x16x32_bf16 v[204:207], a[136:139], v[100:103], v[204:207]\n"
    "ds_read_b64_tr_b16 v[92:93], v21 offset:36864\n"
    "v_add_f32_dpp v230, v230, v230 row_ror:4 row_mask:0xf bank_mask:0xf\n"
    "s_waitcnt lgkmcnt(9)\n"
    "v_mfma_f32_16x16x32_bf16 v[176:179], a[128:131], v[104:107], v[176:179]\n"
    "ds_read_b64_tr_b16 v[94:95], v21 offset:38912\n"
    "v_add_f32_dpp v231, v231, v231 row_ror:4 row_mask:0xf bank_mask:0xf\n"
    "v_mfma_f32_16x16x32_bf16 v[192:195], a[132:135], v[104:107], v[192:195]\n"
    "s_add_u32 m0, s8, 65536\n"
    "s_add_u32 s9, s7, 2048\n"
    "buffer_load_dwordx4 v240, s[32:35], s9 offen offset:0 sc1 lds\n"
    "v_mfma_f32_16x16x32_bf16 v[208:211], a[136:139], v[104:107], v[208:211]\n"
    "buffer_load_dwordx4 v241, s[32:35], s9 offen offset:1024 sc1 lds\n"
    "s_waitcnt lgkmcnt(8)\n"
    "v_mfma_f32_16x16x32_bf16 v[180:183], a[128:131], v[108:111], v[180:183]\n"
    "v_mfma_f32_16x16x32_bf16 v[196:199], a[132:135], v[108:111], v[196:199]\n"
    "buffer_load_dwordx4 v242, s[32:35], s9 offen offset:2048 sc1 lds\n"
    "v_mfma_f32_16x16x32_bf16 v[212:215], a[136:139], v[108:111], v[212:215]\n"
    "buffer_load_dwordx4 v243, s[32:35], s9 offen offset:3072 sc1 lds\n"
    "s_waitcnt lgkmcnt(6)\n"
    "v_mfma_f32_16x16x32_bf16 v[6:9], v[58:61], v[80:83], v[6:9]\n"
    "ds_read_b64_tr_b16 v[96:97], v18 offset:40960\n"
    "v_add_f32_dpp v232, v232, v232 row_ror:4 row_mask:0xf bank_mask:0xf\n"
    "v_mfma_f32_16x16x32_bf16 v[168:171], a[144:147], v[80:83], v[168:171]\n"
    "ds_read_b64_tr_b16 v[98:99], v18 offset:43008\n"
    "v_add_f32_dpp v233, v233, v233 row_ror:4 row_mask:0xf bank_mask:0xf\n"
    "v_mfma_f32_16x16x32_bf16 v[184:187], a[148:151], v[80:83], v[184:187]\n"
    "ds_read_b64_tr_b16 v[100:101], v19 offset:40960\n"
    "v_add_f32_dpp v234, v234, v234 row_ror:4 row_mask:0xf bank_mask:0xf\n"
    "v_mfma_f32_16x16x32_bf16 v[200:203], a[152:155], v[80:83], v[200:203]\n"
    "ds_read_b64_tr_b16 v[102:103], v19 offset:43008\n"
    "v_add_f32_dpp v235, v235, v235 row_ror:4 row_mask:0xf bank_mask:0xf\n"
    "s_waitcnt lgkmcnt(8)\n"
    "v_mfma_f32_16x16x32_bf16 v[172:175], a[144:147], v[84:87], v[172:175]\n"
    "ds_read_b64_tr_b16 v[104:105], v20 offset:40960\n"
    "v_add_f32_dpp v236, v236, v236 row_ror:4 row_mask:0xf bank_mask:0xf\n"
    "v_mfma_f32_16x16x32_bf16 v[188:191], a[148:151], v[84:87], v[188:191]\n"
    "ds_read_b64_tr_b16 v[106:107], v20 offset:43008\n"
    "v_add_f32_dpp v237, v237, v237 row_ror:4 row_mask:0xf bank_mask:0xf\n"
    "v_mfma_f32_16x16x32_bf16 v[204:207], a[152:155], v[84:87], v[204:207]\n"
    "ds_read_b64_tr_b16 v[108:109], v21 offset:40960\n"
    "v_add_f32_dpp v238, v238, v238 row_ror:4 row_mask:0xf bank_mask:0xf\n"
    "s_waitcnt lgkmcnt(9)\n"
    "v_mfma_f32_16x16x32_bf16 v[176:179], a[144:147], v[88:91], v[176:179]\n"
    "ds_read_b64_tr_b16 v[110:111], v21 offset:43008\n"
    "v_add_f32_dpp v239, v239, v239 row_ror:4 row_mask:0xf bank_mask:0xf\n"
    "v_mfma_f32_16x16x32_bf16 v[192:195], a[148:151], v[88:91], v[192:195]\n"
    "s_add_u32 m0, s8, 73728\n"
    "s_add_u32 s9, s7, 264192\n"
    "buffer_load_dwordx4 v240, s[32:35], s9 offen offset:0 sc1 lds\n"
    "v_mfma_f32_16x16x32_bf16 v[208:211], a[152:155], v[88:91], v[208:211]\n"
    "buffer_load_dwordx4 v241, s[32:35], s9 offen offset:1024 sc1 lds\n"
    "ds_write_b128 v250, v[72:75] offset:0\n"
    "s_waitcnt lgkmcnt(9)\n"
    "v_mfma_f32_16x16x32_bf16 v[180:183], a[144:147], v[92:95], v[180:183]\n"
    "v_mfma_f32_16x16x32_bf16 v[196:199], a[148:151], v[92:95], v[196:199]\n"
    "buffer_load_dwordx4 v242, s[32:35], s9 offen offset:2048 sc1 lds\n"
    "ds_write_b128 v250, v[76:79] offset:1024\n"
    "v_mfma_f32_16x16x32_bf16 v[212:215], a[152:155], v[92:95], v[212:215]\n"
    "buffer_load_dwordx4 v243, s[32:35], s9 offen offset:3072 sc1 lds\n"
    "s_waitcnt lgkmcnt(8)\n"
    "v_mfma_f32_16x16x32_bf16 v[6:9], v[58:61], v[96:99], v[6:9]\n"
    "ds_read_b64_tr_b16 v[80:81], v18 offset:45056\n"
    "v_add_f32_dpp v224, v224, v224 row_ror:2 row_mask:0xf bank_mask:0xf\n"
    "v_mfma_f32_16x16x32_bf16 v[168:171], a[160:163], v[96:99], v[168:171]\n"
    "ds_read_b64_tr_b16 v[82:83], v18 offset:47104\n"
    "v_add_f32_dpp v225, v225, v225 row_ror:2 row_mask:0xf bank_mask:0xf\n"
    "v_mfma_f32_16x16x32_bf16 v[184:187], a[164:167], v[96:99], v[184:187]\n"
    "ds_read_b64_tr_b16 v[84:85], v19 offset:45056\n"
    "v_add_f32_dpp v226, v226, v226 row_ror:2 row_mask:0xf bank_mask:0xf\n"
    "s_waitcnt vmcnt(19)\n"
    "v_mfma_f32_16x16x32_bf16 v[200:203], a[168:171], v[96:99], v[200:203]\n"
    "ds_read_b64_tr_b16 v[86:87], v19 offset:47104\n"
    "v_add_f32_dpp v227, v227, v227 row_ror:2 row_mask:0xf bank_mask:0xf\n"
    "s_waitcnt lgkmcnt(10)\n"
    "v_mfma_f32_16x16x32_bf16 v[172:175], a[160:163], v[100:103], v[172:175]\n"
    "ds_read_b64_tr_b16 v[88:89], v20 offset:45056\n"
    "v_add_f32_dpp v228, v228, v228 row_ror:2 row_mask:0xf bank_mask:0xf\n"
    "v_mfma_f32_16x16x32_bf16 v[188:191], a[164:167], v[100:103], v[188:191]\n"
    "ds_read_b64_tr_b16 v[90:91], v20 offset:47104\n"
    "v_add_f32_dpp v229, v229, v229 row_ror:2 row_mask:0xf bank_mask:0xf\n"
    "v_mfma_f32_16x16x32_bf16 v[204:207], a[168:171], v[100:103], v[204:207]\n"
    "ds_read_b64_tr_b16 v[92:93], v21 offset:45056\n"
    "v_add_f32_dpp v230, v230, v230 row_ror:2 row_mask:0xf bank_mask:0xf\n"
    "s_waitcnt lgkmcnt(11)\n"
    "v_mfma_f32_16x16x32_bf16 v[176:179], a[160:163], v[104:107], v[176:179]\n"
    "ds_read_b64_tr_b16 v[94:95], v21 offset:47104\n"
    "v_add_f32_dpp v231, v231, v231 row_ror:2 row_mask:0xf bank_mask:0xf\n"
    "v_mfma_f32_16x16x32_bf16 v[192:195], a[164:167], v[104:107], v[192:195]\n"
    "buffer_load_dwordx4 a[232:235], v29, s[32:35], s42 offen offset:2048 sc1\n"
    "v_mfma_f32_16x16x32_bf16 v[208:211], a[168:171], v[104:107], v[208:211]\n"
    "buffer_load_dwordx4 a[240:243], v29, s[32:35], s40 offen offset:3072 sc1\n"
    "s_waitcnt lgkmcnt(10)\n"
    "v_mfma_f32_16x16x32_bf16 v[180:183], a[160:163], v[108:111], v[180:183]\n"
    "v_mfma_f32_16x16x32_bf16 v[196:199], a[164:167], v[108:111], v[196:199]\n"
    "buffer_load_dwordx4 a[244:247], v29, s[32:35], s41 offen offset:3072 sc1\n"
    "v_mfma_f32_16x16x32_bf16 v[212:215], a[168:171], v[108:111], v[212:215]\n"
    "buffer_load_dwordx4 a[248:251], v29, s[32:35], s42 offen offset:3072 sc1\n"
    "s_waitcnt lgkmcnt(6)\n"
    "v_mfma_f32_16x16x32_bf16 v[6:9], v[58:61], v[80:83], v[6:9]\n"
    "ds_read_b64_tr_b16 v[96:97], v18 offset:49152\n"
    "v_add_f32_dpp v232, v232, v232 row_ror:2 row_mask:0xf bank_mask:0xf\n"
    "s_waitcnt vmcnt(22)\n"
    "v_mfma_f32_16x16x32_bf16 v[168:171], a[176:179], v[80:83], v[168:171]\n"
    "ds_read_b64_tr_b16 v[98:99], v18 offset:51200\n"
    "v_add_f32_dpp v233, v233, v233 row_ror:2 row_mask:0xf bank_mask:0xf\n"
    "s_waitcnt vmcnt(21)\n"
    "v_mfma_f32_16x16x32_bf16 v[184:187], a[180:183], v[80:83], v[184:187]\n"
    "ds_read_b64_tr_b16 v[100:101], v19 offset:49152\n"
    "v_add_f32_dpp v234, v234, v234 row_ror:2 row_mask:0xf bank_mask:0xf\n"
    "s_waitcnt vmcnt(20)\n"
    "v_mfma_f32_16x16x32_bf16 v[200:203], a[184:187], v[80:83], v[200:203]\n"
    "ds_read_b64_tr_b16 v[102:103], v19 offset:51200\n"
    "v_add_f32_dpp v235, v235, v235 row_ror:2 row_mask:0xf bank_mask:0xf\n"
    "s_waitcnt lgkmcnt(8)\n"
    "v_mfma_f32_16x16x32_bf16 v[172:175], a[176:179], v[84:87], v[172:175]\n"
    "ds_read_b64_tr_b16 v[104:105], v20 offset:49152\n"
    "v_add_f32_dpp v236, v236, v236 row_ror:2 row_mask:0xf bank_mask:0xf\n"
    "v_mfma_f32_16x16x32_bf16 v[188:191], a[180:183], v[84:87], v[188:191]\n"
    "ds_read_b64_tr_b16 v[106:107], v20 offset:51200\n"
    "v_add_f32_dpp v237, v237, v237 row_ror:2 row_mask:0xf bank_mask:0xf\n"
    "v_mfma_f32_16x16x32_bf16 v[204:207], a[184:187], v[84:87], v[204:207]\n"
    "ds_read_b64_tr_b16 v[108:109], v21 offset:49152\n"
    "v_add_f32_dpp v238, v238, v238 row_ror:2 row_mask:0xf bank_mask:0xf\n"
    "s_waitcnt lgkmcnt(9)\n"
    "v_mfma_f32_16x16x32_bf16 v[176:179], a[176:179], v[88:91], v[176:179]\n"
    "ds_read_b64_tr_b16 v[110:111], v21 offset:51200\n"
    "v_add_f32_dpp v239, v239, v239 row_ror:2 row_mask:0xf bank_mask:0xf\n"
    "v_mfma_f32_16x16x32_bf16 v[192:195], a[180:183], v[88:91], v[192:195]\n"
    "s_add_u32 m0, s8, 81920\n"
    "s_add_u32 s9, s7, 526336\n"
    "buffer_load_dwordx4 v240, s[32:35], s9 offen offset:0 sc1 lds\n"
    "v_mfma_f32_16x16x32_bf16 v[208:211], a[184:187], v[88:91], v[208:211]\n"
    "buffer_load_dwordx4 v241, s[32:35], s9 offen offset:1024 sc1 lds\n"
    "s_waitcnt lgkmcnt(8)\n"
    "v_mfma_f32_16x16x32_bf16 v[180:183], a[176:179], v[92:95], v[180:183]\n"
    "v_mfma_f32_16x16x32_bf16 v[196:199], a[180:183], v[92:95], v[196:199]\n"
    "buffer_load_dwordx4 v242, s[32:35], s9 offen offset:2048 sc1 lds\n"
    "v_mfma_f32_16x16x32_bf16 v[212:215], a[184:187], v[92:95], v[212:215]\n"
    "buffer_load_dwordx4 v243, s[32:35], s9 offen offset:3072 sc1 lds\n"
    "s_waitcnt lgkmcnt(6)\n"
    "v_mfma_f32_16x16x32_bf16 v[6:9], v[58:61], v[96:99], v[6:9]\n"
    "ds_read_b64_tr_b16 v[80:81], v18 offset:53248\n"
    "v_add_f32_dpp v224, v224, v224 row_ror:1 row_mask:0xf bank_mask:0xf\n"
    "s_waitcnt vmcnt(23)\n"
    "v_mfma_f32_16x16x32_bf16 v[168:171], a[192:195], v[96:99], v[168:171]\n"
    "ds_read_b64_tr_b16 v[82:83], v18 offset:55296\n"
    "v_add_f32_dpp v225, v225, v225 row_ror:1 row_mask:0xf bank_mask:0xf\n"
    "s_waitcnt vmcnt(22)\n"
    "v_mfma_f32_16x16x32_bf16 v[184:187], a[196:199], v[96:99], v[184:187]\n"
    "ds_read_b64_tr_b16 v[84:85], v19 offset:53248\n"
    "v_add_f32_dpp v226, v226, v226 row_ror:1 row_mask:0xf bank_mask:0xf\n"
    "s_waitcnt vmcnt(21)\n"
    "v_mfma_f32_16x16x32_bf16 v[200:203], a[200:203], v[96:99], v[200:203]\n"
    "ds_read_b64_tr_b16 v[86:87], v19 offset:55296\n"
    "v_add_f32_dpp v227, v227, v227 row_ror:1 row_mask:0xf bank_mask:0xf\n"
    "s_waitcnt lgkmcnt(8)\n"
    "v_mfma_f32_16x16x32_bf16 v[172:175], a[192:195], v[100:103], v[172:175]\n"
    "ds_read_b64_tr_b16 v[88:89], v20 offset:53248\n"
    "v_add_f32_dpp v228, v228, v228 row_ror:1 row_mask:0xf bank_mask:0xf\n"
    "v_mfma_f32_16x16x32_bf16 v[188:191], a[196:199], v[100:103], v[188:191]\n"
    "ds_read_b64_tr_b16 v[90:91], v20 offset:55296\n"
    "v_add_f32_dpp v229, v229, v229 row_ror:1 row_mask:0xf bank_mask:0xf\n"
    "v_mfma_f32_16x16x32_bf16 v[204:207], a[200:203], v[100:103], v[204:207]\n"
    "ds_read_b64_tr_b16 v[92:93], v21 offset:53248\n"
    "v_add_f32_dpp v230, v230, v230 row_ror:1 row_mask:0xf bank_mask:0xf\n"
    "s_waitcnt lgkmcnt(9)\n"
    "v_mfma_f32_16x16x32_bf16 v[176:179], a[192:195], v[104:107], v[176:179]\n"
    "ds_read_b64_tr_b16 v[94:95], v21 offset:55296\n"
    "v_add_f32_dpp v231, v231, v231 row_ror:1 row_mask:0xf bank_mask:0xf\n"
    "v_mfma_f32_16x16x32_bf16 v[192:195], a[196:199], v[104:107], v[192:195]\n"
    "s_add_u32 m0, s8, 90112\n"
    "s_add_u32 s9, s7, 788480\n"
    "buffer_load_dwordx4 v240, s[32:35], s9 offen offset:0 sc1 lds\n"
    "v_mfma_f32_16x16x32_bf16 v[208:211], a[200:203], v[104:107], v[208:211]\n"
    "buffer_load_dwordx4 v241, s[32:35], s9 offen offset:1024 sc1 lds\n"
    "s_waitcnt lgkmcnt(8)\n"
    "v_mfma_f32_16x16x32_bf16 v[180:183], a[192:195], v[108:111], v[180:183]\n"
    "v_mfma_f32_16x16x32_bf16 v[196:199], a[196:199], v[108:111], v[196:199]\n"
    "buffer_load_dwordx4 v242, s[32:35], s9 offen offset:2048 sc1 lds\n"
    "v_mfma_f32_16x16x32_bf16 v[212:215], a[200:203], v[108:111], v[212:215]\n"
    "buffer_load_dwordx4 v243, s[32:35], s9 offen offset:3072 sc1 lds\n"
    "s_waitcnt lgkmcnt(6)\n"
    "v_mfma_f32_16x16x32_bf16 v[6:9], v[58:61], v[80:83], v[6:9]\n"
    "ds_read_b64_tr_b16 v[96:97], v18 offset:57344\n"
    "v_add_f32_dpp v232, v232, v232 row_ror:1 row_mask:0xf bank_mask:0xf\n"
    "s_waitcnt vmcnt(24)\n"
    "v_mfma_f32_16x16x32_bf16 v[168:171], a[208:211], v[80:83], v[168:171]\n"
    "ds_read_b64_tr_b16 v[98:99], v18 offset:59392\n"
    "v_add_f32_dpp v233, v233, v233 row_ror:1 row_mask:0xf bank_mask:0xf\n"
    "s_waitcnt vmcnt(23)\n"
    "v_mfma_f32_16x16x32_bf16 v[184:187], a[212:215], v[80:83], v[184:187]\n"
    "ds_read_b64_tr_b16 v[100:101], v19 offset:57344\n"
    "v_add_f32_dpp v234, v234, v234 row_ror:1 row_mask:0xf bank_mask:0xf\n"
    "s_waitcnt vmcnt(22)\n"
    "v_mfma_f32_16x16x32_bf16 v[200:203], a[216:219], v[80:83], v[200:203]\n"
    "ds_read_b64_tr_b16 v[102:103], v19 offset:59392\n"
    "v_add_f32_dpp v235, v235, v235 row_ror:1 row_mask:0xf bank_mask:0xf\n"
    "s_waitcnt lgkmcnt(8)\n"
    "v_mfma_f32_16x16x32_bf16 v[172:175], a[208:211], v[84:87], v[172:175]\n"
    "ds_read_b64_tr_b16 v[104:105], v20 offset:57344\n"
    "v_add_f32_dpp v236, v236, v236 row_ror:1 row_mask:0xf bank_mask:0xf\n"
    "v_mfma_f32_16x16x32_bf16 v[188:191], a[212:215], v[84:87], v[188:191]\n"
    "ds_read_b64_tr_b16 v[106:107], v20 offset:59392\n"
    "v_add_f32_dpp v237, v237, v237 row_ror:1 row_mask:0xf bank_mask:0xf\n"
    "v_mfma_f32_16x16x32_bf16 v[204:207], a[216:219], v[84:87], v[204:207]\n"
    "ds_read_b64_tr_b16 v[108:109], v21 offset:57344\n"
    "v_add_f32_dpp v238, v238, v238 row_ror:1 row_mask:0xf bank_mask:0xf\n"
    "s_waitcnt lgkmcnt(9)\n"
    "v_mfma_f32_16x16x32_bf16 v[176:179], a[208:211], v[88:91], v[176:179]\n"
    "ds_read_b64_tr_b16 v[110:111], v21 offset:59392\n"
    "v_add_f32_dpp v239, v239, v239 row_ror:1 row_mask:0xf bank_mask:0xf\n"
    "v_mfma_f32_16x16x32_bf16 v[192:195], a[212:215], v[88:91], v[192:195]\n"
    "s_lshl_b32 s67, s56, 10\n"
    "s_add_u32 m0, s67, 147456\n"
    "s_mov_b32 s68, 16777216\n"
    "buffer_load_dwordx4 v56, s[32:35], s68 offen sc1 lds\n"
    "v_mfma_f32_16x16x32_bf16 v[208:211], a[216:219], v[88:91], v[208:211]\n"
    "s_lshl_b32 s67, s56, 10\n"
    "s_add_u32 m0, s67, 151552\n"
    "s_mov_b32 s68, 16779264\n"
    "buffer_load_dwordx4 v56, s[32:35], s68 offen sc1 lds\n"
    "s_waitcnt lgkmcnt(8)\n"
    "v_mfma_f32_16x16x32_bf16 v[180:183], a[208:211], v[92:95], v[180:183]\n"
    "v_mfma_f32_16x16x32_bf16 v[196:199], a[212:215], v[92:95], v[196:199]\n"
    "v_mfma_f32_16x16x32_bf16 v[212:215], a[216:219], v[92:95], v[212:215]\n"
    "s_waitcnt lgkmcnt(6)\n"
    "v_mfma_f32_16x16x32_bf16 v[6:9], v[58:61], v[96:99], v[6:9]\n"
    "ds_read_b64_tr_b16 v[80:81], v18 offset:61440\n"
    "s_waitcnt vmcnt(23)\n"
    "v_mfma_f32_16x16x32_bf16 v[168:171], a[224:227], v[96:99], v[168:171]\n"
    "ds_read_b64_tr_b16 v[82:83], v18 offset:63488\n"
    "s_waitcnt vmcnt(22)\n"
    "v_mfma_f32_16x16x32_bf16 v[184:187], a[228:231], v[96:99], v[184:187]\n"
    "ds_read_b64_tr_b16 v[84:85], v19 offset:61440\n"
    "s_waitcnt vmcnt(13)\n"
    "v_mfma_f32_16x16x32_bf16 v[200:203], a[232:235], v[96:99], v[200:203]\n"
    "ds_read_b64_tr_b16 v[86:87], v19 offset:63488\n"
    "s_waitcnt lgkmcnt(8)\n"
    "v_mfma_f32_16x16x32_bf16 v[172:175], a[224:227], v[100:103], v[172:175]\n"
    "ds_read_b64_tr_b16 v[88:89], v20 offset:61440\n"
    "v_mfma_f32_16x16x32_bf16 v[188:191], a[228:231], v[100:103], v[188:191]\n"
    "ds_read_b64_tr_b16 v[90:91], v20 offset:63488\n"
    "v_mfma_f32_16x16x32_bf16 v[204:207], a[232:235], v[100:103], v[204:207]\n"
    "ds_read_b64_tr_b16 v[92:93], v21 offset:61440\n"
    "s_waitcnt lgkmcnt(9)\n"
    "v_mfma_f32_16x16x32_bf16 v[176:179], a[224:227], v[104:107], v[176:179]\n"
    "ds_read_b64_tr_b16 v[94:95], v21 offset:63488\n"
    "v_mfma_f32_16x16x32_bf16 v[192:195], a[228:231], v[104:107], v[192:195]\n"
    "v_mfma_f32_16x16x32_bf16 v[208:211], a[232:235], v[104:107], v[208:211]\n"
    "s_waitcnt lgkmcnt(8)\n"
    "v_mfma_f32_16x16x32_bf16 v[180:183], a[224:227], v[108:111], v[180:183]\n"
    "v_mfma_f32_16x16x32_bf16 v[196:199], a[228:231], v[108:111], v[196:199]\n"
    "v_mfma_f32_16x16x32_bf16 v[212:215], a[232:235], v[108:111], v[212:215]\n"
    "s_barrier\n"
    "v_add_u32 v18, s49, v18\n"
    "v_add_u32 v19, s49, v19\n"
    "v_add_u32 v20, s49, v20\n"
    "v_add_u32 v21, s49, v21\n"
    "s_waitcnt lgkmcnt(6)\n"
    "v_mfma_f32_16x16x32_bf16 v[6:9], v[58:61], v[80:83], v[6:9]\n"
    "ds_read_b64_tr_b16 v[96:97], v18 offset:0\n"
    "s_waitcnt vmcnt(12)\n"
    "v_mfma_f32_16x16x32_bf16 v[168:171], a[240:243], v[80:83], v[168:171]\n"
    "ds_read_b64_tr_b16 v[98:99], v18 offset:2048\n"
    "s_waitcnt vmcnt(11)\n"
    "v_mfma_f32_16x16x32_bf16 v[184:187], a[244:247], v[80:83], v[184:187]\n"
    "ds_read_b64_tr_b16 v[100:101], v19 offset:0\n"
    "s_waitcnt vmcnt(10)\n"
    "v_mfma_f32_16x16x32_bf16 v[200:203], a[248:251], v[80:83], v[200:203]\n"
    "ds_read_b64_tr_b16 v[102:103], v19 offset:2048\n"
    "s_waitcnt lgkmcnt(8)\n"
    "v_mfma_f32_16x16x32_bf16 v[172:175], a[240:243], v[84:87], v[172:175]\n"
    "ds_read_b64_tr_b16 v[104:105], v20 offset:0\n"
    "v_mfma_f32_16x16x32_bf16 v[188:191], a[244:247], v[84:87], v[188:191]\n"
    "ds_read_b64_tr_b16 v[106:107], v20 offset:2048\n"
    "v_mfma_f32_16x16x32_bf16 v[204:207], a[248:251], v[84:87], v[204:207]\n"
    "ds_read_b64_tr_b16 v[108:109], v21 offset:0\n"
    "s_waitcnt lgkmcnt(9)\n"
    "v_mfma_f32_16x16x32_bf16 v[176:179], a[240:243], v[88:91], v[176:179]\n"
    "ds_read_b64_tr_b16 v[110:111], v21 offset:2048\n"
    "v_mfma_f32_16x16x32_bf16 v[192:195], a[244:247], v[88:91], v[192:195]\n"
    "v_mul_f32 v2, s26, v6\n"
    "v_cvt_pk_bf16_f32 v2, v2, v2\n"
    "v_lshlrev_b32 v2, 16, v2\n"
    "v_mfma_f32_16x16x32_bf16 v[208:211], a[248:251], v[88:91], v[208:211]\n"
    "ds_write_b32 v23, v2 offset:0\n"
    "v_mov_b32 v17, v6\n"
    "v_cndmask_b32 v16, v252, v253, s[24:25]\n"
    "s_waitcnt lgkmcnt(9)\n"
    "v_mfma_f32_16x16x32_bf16 v[180:183], a[240:243], v[92:95], v[180:183]\n"
    "v_cndmask_b32 v16, v16, v254, s[30:31]\n"
    "v_cndmask_b32 v16, v16, v255, s[44:45]\n"
    "s_waitcnt lgkmcnt(0)\n"
    "s_barrier\n"
    "v_mfma_f32_16x16x32_bf16 v[196:199], a[244:247], v[92:95], v[196:199]\n"
    "ds_read_b32 v34, v46 offset:0\n"
    "ds_read_b32 v35, v47 offset:0\n"
    "ds_read_b32 v36, v48 offset:0\n"
    "v_mfma_f32_16x16x32_bf16 v[212:215], a[248:251], v[92:95], v[212:215]\n"
    "ds_read_b32 v37, v49 offset:0\n"
    "ds_read_b128 v[72:75], v251 offset:0\n"
    "ds_read_b128 v[76:79], v251 offset:1024\n"
    "v_add_u32 v10, 65536, v10\n"
    "s_waitcnt lgkmcnt(4)\n"
    "v_pk_fma_f32 v[168:169], v[34:35], v[224:225], v[168:169] op_sel:[0,0,0] op_sel_hi:[0,1,1] neg_lo:[1,0,0] neg_hi:[1,0,0]\n"
    "s_mov_b32 s21, 14417920\n"
    "buffer_load_dwordx4 a[140:143], v52, s[32:35], s21 offen offset:1024 sc1\n"
    "v_pk_fma_f32 v[170:171], v[34:35], v[226:227], v[170:171] op_sel:[0,0,0] op_sel_hi:[0,1,1] neg_lo:[1,0,0] neg_hi:[1,0,0]\n"
    "v_pk_mul_f32 v[168:169], v[168:169], v[170:171]\n"
    "v_cvt_pk_bf16_f32 v38, v168, v169\n"
    "v_pk_fma_f32 v[172:173], v[34:35], v[224:225], v[172:173] op_sel:[1,0,0] op_sel_hi:[1,1,1] neg_lo:[1,0,0] neg_hi:[1,0,0]\n"
    "v_pk_fma_f32 v[174:175], v[34:35], v[226:227], v[174:175] op_sel:[1,0,0] op_sel_hi:[1,1,1] neg_lo:[1,0,0] neg_hi:[1,0,0]\n"
    "v_pk_mul_f32 v[172:173], v[172:173], v[174:175]\n"
    "s_mov_b32 s21, 14450688\n"
    "buffer_load_dwordx4 a[156:159], v52, s[32:35], s21 offen offset:1024 sc1\n"
    "v_cvt_pk_bf16_f32 v39, v172, v173\n"
    "s_waitcnt lgkmcnt(2)\n"
    "v_pk_fma_f32 v[176:177], v[36:37], v[224:225], v[176:177] op_sel:[0,0,0] op_sel_hi:[0,1,1] neg_lo:[1,0,0] neg_hi:[1,0,0]\n"
    "v_pk_fma_f32 v[178:179], v[36:37], v[226:227], v[178:179] op_sel:[0,0,0] op_sel_hi:[0,1,1] neg_lo:[1,0,0] neg_hi:[1,0,0]\n"
    "v_pk_mul_f32 v[176:177], v[176:177], v[178:179]\n"
    "v_cvt_pk_bf16_f32 v40, v176, v177\n"
    "v_pk_fma_f32 v[180:181], v[36:37], v[224:225], v[180:181] op_sel:[1,0,0] op_sel_hi:[1,1,1] neg_lo:[1,0,0] neg_hi:[1,0,0]\n"
    "v_pk_fma_f32 v[182:183], v[36:37], v[226:227], v[182:183] op_sel:[1,0,0] op_sel_hi:[1,1,1] neg_lo:[1,0,0] neg_hi:[1,0,0]\n"
    "s_mov_b32 s21, 14483456\n"
    "buffer_load_dwordx4 a[172:175], v52, s[32:35], s21 offen offset:1024 sc1\n"
    "v_pk_mul_f32 v[180:181], v[180:181], v[182:183]\n"
    "v_cvt_pk_bf16_f32 v41, v180, v181\n"
    "v_permlane32_swap_b32 v38, v40\n"
    "s_nop 0\n"
    "v_permlane32_swap_b32 v39, v41\n"
    "s_nop 1\n"
    "v_permlane16_swap_b32 v38, v39\n"
    "v_permlane16_swap_b32 v40, v41\n"
    "s_add_u32 s22, s12, 0\n"
    "buffer_store_dwordx4 v[38:41], v13, s[32:35], s22 offen offset:0 sc1\n"
    "s_mov_b32 s21, 14516224\n"
    "buffer_load_dwordx4 a[188:191], v52, s[32:35], s21 offen offset:1024 sc1\n"
    "v_pk_fma_f32 v[184:185], v[34:35], v[228:229], v[184:185] op_sel:[0,0,0] op_sel_hi:[0,1,1] neg_lo:[1,0,0] neg_hi:[1,0,0]\n"
    "v_pk_fma_f32 v[186:187], v[34:35], v[230:231], v[186:187] op_sel:[0,0,0] op_sel_hi:[0,1,1] neg_lo:[1,0,0] neg_hi:[1,0,0]\n"
    "v_pk_mul_f32 v[184:185], v[184:185], v[186:187]\n"
    "v_cvt_pk_bf16_f32 v64, v184, v185\n"
    "v_pk_fma_f32 v[188:189], v[34:35], v[228:229], v[188:189] op_sel:[1,0,0] op_sel_hi:[1,1,1] neg_lo:[1,0,0] neg_hi:[1,0,0]\n"
    "v_pk_fma_f32 v[190:191], v[34:35], v[230:231], v[190:191] op_sel:[1,0,0] op_sel_hi:[1,1,1] neg_lo:[1,0,0] neg_hi:[1,0,0]\n"
    "s_mov_b32 s21, 14548992\n"
    "buffer_load_dwordx4 a[204:207], v52, s[32:35], s21 offen offset:1024 sc1\n"
    "v_pk_mul_f32 v[188:189], v[188:189], v[190:191]\n"
    "v_cvt_pk_bf16_f32 v65, v188, v189\n"
    "v_pk_fma_f32 v[192:193], v[36:37], v[228:229], v[192:193] op_sel:[0,0,0] op_sel_hi:[0,1,1] neg_lo:[1,0,0] neg_hi:[1,0,0]\n"
    "v_pk_fma_f32 v[194:195], v[36:37], v[230:231], v[194:195] op_sel:[0,0,0] op_sel_hi:[0,1,1] neg_lo:[1,0,0] neg_hi:[1,0,0]\n"
    "v_pk_mul_f32 v[192:193], v[192:193], v[194:195]\n"
    "v_cvt_pk_bf16_f32 v66, v192, v193\n"
    "v_pk_fma_f32 v[196:197], v[36:37], v[228:229], v[196:197] op_sel:[1,0,0] op_sel_hi:[1,1,1] neg_lo:[1,0,0] neg_hi:[1,0,0]\n"
    "s_mov_b32 s21, 14581760\n"
    "buffer_load_dwordx4 a[220:223], v52, s[32:35], s21 offen offset:1024 sc1\n"
    "v_pk_fma_f32 v[198:199], v[36:37], v[230:231], v[198:199] op_sel:[1,0,0] op_sel_hi:[1,1,1] neg_lo:[1,0,0] neg_hi:[1,0,0]\n"
    "v_pk_mul_f32 v[196:197], v[196:197], v[198:199]\n"
    "v_cvt_pk_bf16_f32 v67, v196, v197\n"
    "v_permlane32_swap_b32 v64, v66\n"
    "s_nop 0\n"
    "v_permlane32_swap_b32 v65, v67\n"
    "s_nop 1\n"
    "v_permlane16_swap_b32 v64, v65\n"
    "v_permlane16_swap_b32 v66, v67\n"
    "buffer_store_dwordx4 v[64:67], v13, s[32:35], s22 offen offset:1024 sc1\n"
    "v_pk_fma_f32 v[200:201], v[34:35], v[232:233], v[200:201] op_sel:[0,0,0] op_sel_hi:[0,1,1] neg_lo:[1,0,0] neg_hi:[1,0,0]\n"
    "v_pk_fma_f32 v[202:203], v[34:35], v[234:235], v[202:203] op_sel:[0,0,0] op_sel_hi:[0,1,1] neg_lo:[1,0,0] neg_hi:[1,0,0]\n"
    "v_pk_mul_f32 v[200:201], v[200:201], v[202:203]\n"
    "v_cvt_pk_bf16_f32 v38, v200, v201\n"
    "v_pk_fma_f32 v[204:205], v[34:35], v[232:233], v[204:205] op_sel:[1,0,0] op_sel_hi:[1,1,1] neg_lo:[1,0,0] neg_hi:[1,0,0]\n"
    "v_pk_fma_f32 v[206:207], v[34:35], v[234:235], v[206:207] op_sel:[1,0,0] op_sel_hi:[1,1,1] neg_lo:[1,0,0] neg_hi:[1,0,0]\n"
    "v_pk_mul_f32 v[204:205], v[204:205], v[206:207]\n"
    "v_cvt_pk_bf16_f32 v39, v204, v205\n"
    "v_pk_fma_f32 v[208:209], v[36:37], v[232:233], v[208:209] op_sel:[0,0,0] op_sel_hi:[0,1,1] neg_lo:[1,0,0] neg_hi:[1,0,0]\n"
    "v_pk_fma_f32 v[210:211], v[36:37], v[234:235], v[210:211] op_sel:[0,0,0] op_sel_hi:[0,1,1] neg_lo:[1,0,0] neg_hi:[1,0,0]\n"
    "v_pk_mul_f32 v[208:209], v[208:209], v[210:211]\n"
    "v_cvt_pk_bf16_f32 v40, v208, v209\n"
    "v_pk_fma_f32 v[212:213], v[36:37], v[232:233], v[212:213] op_sel:[1,0,0] op_sel_hi:[1,1,1] neg_lo:[1,0,0] neg_hi:[1,0,0]\n"
    "v_pk_fma_f32 v[214:215], v[36:37], v[234:235], v[214:215] op_sel:[1,0,0] op_sel_hi:[1,1,1] neg_lo:[1,0,0] neg_hi:[1,0,0]\n"
    "v_pk_mul_f32 v[212:213], v[212:213], v[214:215]\n"
    "v_cvt_pk_bf16_f32 v41, v212, v213\n"
    "v_permlane32_swap_b32 v38, v40\n"
    "s_nop 0\n"
    "v_permlane32_swap_b32 v39, v41\n"
    "s_nop 1\n"
    "v_permlane16_swap_b32 v38, v39\n"
    "v_permlane16_swap_b32 v40, v41\n"
    "buffer_store_dwordx4 v[38:41], v13, s[32:35], s22 offen offset:2048 sc1\n"
    "s_waitcnt lgkmcnt(1)\n"
    "v_pk_add_f32 v[216:217], v[216:217], v[72:73]\n"
    "v_pk_add_f32 v[218:219], v[218:219], v[74:75]\n"
    "v_pk_fma_f32 v[216:217], v[34:35], v[236:237], v[216:217] op_sel:[0,0,0] op_sel_hi:[0,1,1] neg_lo:[1,0,0] neg_hi:[1,0,0]\n"
    "v_pk_fma_f32 v[218:219], v[34:35], v[238:239], v[218:219] op_sel:[0,0,0] op_sel_hi:[0,1,1] neg_lo:[1,0,0] neg_hi:[1,0,0]\n"
    "v_pk_mul_f32 v[216:217], v[216:217], v[218:219]\n"
    "v_cvt_pk_bf16_f32 v64, v216, v217\n"
    "s_waitcnt lgkmcnt(0)\n"
    "v_pk_add_f32 v[220:221], v[220:221], v[76:77]\n"
    "v_pk_add_f32 v[222:223], v[222:223], v[78:79]\n"
    "v_pk_fma_f32 v[220:221], v[34:35], v[236:237], v[220:221] op_sel:[1,0,0] op_sel_hi:[1,1,1] neg_lo:[1,0,0] neg_hi:[1,0,0]\n"
    "v_pk_fma_f32 v[222:223], v[34:35], v[238:239], v[222:223] op_sel:[1,0,0] op_sel_hi:[1,1,1] neg_lo:[1,0,0] neg_hi:[1,0,0]\n"
    "v_pk_mul_f32 v[220:221], v[220:221], v[222:223]\n"
    "v_cvt_pk_bf16_f32 v65, v220, v221\n"
    "v_permlane32_swap_b32 v64, v66\n"
    "s_nop 0\n"
    "v_permlane32_swap_b32 v65, v67\n"
    "s_nop 1\n"
    "v_permlane16_swap_b32 v64, v65\n"
    "v_permlane16_swap_b32 v66, v67\n"
    "s_add_u32 s22, s16, 0\n"
    "s_mov_b32 exec_hi, 0\n"
    "buffer_store_dwordx4 v[64:67], v13, s[32:35], s22 offen offset:0 sc1\n"
    "s_mov_b32 exec_hi, -1\n"
    "s_add_u32 s67, s17, 0\n"
    "s_mov_b64 exec, s[46:47]\n"
    "buffer_store_dwordx2 v[16:17], v22, s[32:35], s67 offen sc1\n"
    "s_mov_b64 exec, -1\n"
    "v_mfma_f32_16x16x32_bf16 v[6:9], v[58:61], v[96:99], 0\n"
    "ds_read_b64_tr_b16 v[80:81], v18 offset:4096\n"
    "v_mfma_f32_16x16x32_bf16 v[112:115], a[0:3], v[96:99], 0\n"
    "ds_read_b64_tr_b16 v[82:83], v18 offset:6144\n"
    "v_mfma_f32_16x16x32_bf16 v[128:131], a[4:7], v[96:99], 0\n"
    "ds_read_b64_tr_b16 v[84:85], v19 offset:4096\n"
    "v_mfma_f32_16x16x32_bf16 v[144:147], a[8:11], v[96:99], 0\n"
    "ds_read_b64_tr_b16 v[86:87], v19 offset:6144\n"
    "v_mfma_f32_16x16x32_bf16 v[160:163], a[12:15], v[96:99], 0\n"
    "ds_read_b64_tr_b16 v[88:89], v20 offset:4096\n"
    "v_mfma_f32_16x16x32_bf16 v[116:119], a[0:3], v[100:103], 0\n"
    "ds_read_b64_tr_b16 v[90:91], v20 offset:6144\n"
    "v_mfma_f32_16x16x32_bf16 v[132:135], a[4:7], v[100:103], 0\n"
    "ds_read_b64_tr_b16 v[92:93], v21 offset:4096\n"
    "v_mfma_f32_16x16x32_bf16 v[148:151], a[8:11], v[100:103], 0\n"
    "ds_read_b64_tr_b16 v[94:95], v21 offset:6144\n"
    "v_mfma_f32_16x16x32_bf16 v[164:167], a[12:15], v[100:103], 0\n"
    "v_mfma_f32_16x16x32_bf16 v[120:123], a[0:3], v[104:107], 0\n"
    "v_mfma_f32_16x16x32_bf16 v[136:139], a[4:7], v[104:107], 0\n"
    "v_mfma_f32_16x16x32_bf16 v[152:155], a[8:11], v[104:107], 0\n"
    "v_mfma_f32_16x16x32_bf16 v[72:75], a[12:15], v[104:107], 0\n"
    "v_mfma_f32_16x16x32_bf16 v[124:127], a[0:3], v[108:111], 0\n"
    "v_mfma_f32_16x16x32_bf16 v[140:143], a[4:7], v[108:111], 0\n"
    "v_mfma_f32_16x16x32_bf16 v[156:159], a[8:11], v[108:111], 0\n"
    "v_mfma_f32_16x16x32_bf16 v[76:79], a[12:15], v[108:111], 0\n"
    "s_waitcnt lgkmcnt(6)\n"
    "v_mfma_f32_16x16x32_bf16 v[6:9], v[58:61], v[80:83], v[6:9]\n"
    "ds_read_b64_tr_b16 v[96:97], v18 offset:8192\n"
    "v_mfma_f32_16x16x32_bf16 v[112:115], a[16:19], v[80:83], v[112:115]\n"
    "ds_read_b64_tr_b16 v[98:99], v18 offset:10240\n"
    "v_mfma_f32_16x16x32_bf16 v[128:131], a[20:23], v[80:83], v[128:131]\n"
    "ds_read_b64_tr_b16 v[100:101], v19 offset:8192\n"
    "v_mfma_f32_16x16x32_bf16 v[144:147], a[24:27], v[80:83], v[144:147]\n"
    "ds_read_b64_tr_b16 v[102:103], v19 offset:10240\n"
    "v_mfma_f32_16x16x32_bf16 v[160:163], a[28:31], v[80:83], v[160:163]\n"
    "ds_read_b64_tr_b16 v[104:105], v20 offset:8192\n"
    "s_waitcnt lgkmcnt(9)\n"
    "v_mfma_f32_16x16x32_bf16 v[116:119], a[16:19], v[84:87], v[116:119]\n"
    "ds_read_b64_tr_b16 v[106:107], v20 offset:10240\n"
    "v_mfma_f32_16x16x32_bf16 v[132:135], a[20:23], v[84:87], v[132:135]\n"
    "ds_read_b64_tr_b16 v[108:109], v21 offset:8192\n"
    "v_mfma_f32_16x16x32_bf16 v[148:151], a[24:27], v[84:87], v[148:151]\n"
    "ds_read_b64_tr_b16 v[110:111], v21 offset:10240\n"
    "v_mfma_f32_16x16x32_bf16 v[164:167], a[28:31], v[84:87], v[164:167]\n"
    "s_mov_b32 s21, 14155776\n"
    "buffer_load_dwordx4 a[0:3], v51, s[32:35], s21 offen offset:0 sc1\n"
    "s_waitcnt lgkmcnt(10)\n"
    "v_mfma_f32_16x16x32_bf16 v[120:123], a[16:19], v[88:91], v[120:123]\n"
    "v_mfma_f32_16x16x32_bf16 v[136:139], a[20:23], v[88:91], v[136:139]\n"
    "buffer_load_dwordx4 a[4:7], v51, s[32:35], s21 offen offset:1024 sc1\n"
    "v_mfma_f32_16x16x32_bf16 v[152:155], a[24:27], v[88:91], v[152:155]\n"
    "v_mfma_f32_16x16x32_bf16 v[72:75], a[28:31], v[88:91], v[72:75]\n"
    "buffer_load_dwordx4 a[8:11], v52, s[32:35], s21 offen offset:0 sc1\n"
    "s_waitcnt lgkmcnt(8)\n"
    "v_mfma_f32_16x16x32_bf16 v[124:127], a[16:19], v[92:95], v[124:127]\n"
    "v_mfma_f32_16x16x32_bf16 v[140:143], a[20:23], v[92:95], v[140:143]\n"
    "buffer_load_dwordx4 a[12:15], v52, s[32:35], s21 offen offset:1024 sc1\n"
    "v_mfma_f32_16x16x32_bf16 v[156:159], a[24:27], v[92:95], v[156:159]\n"
    "v_mfma_f32_16x16x32_bf16 v[76:79], a[28:31], v[92:95], v[76:79]\n"
    "s_waitcnt lgkmcnt(6)\n"
    "v_mfma_f32_16x16x32_bf16 v[6:9], v[58:61], v[96:99], v[6:9]\n"
    "ds_read_b64_tr_b16 v[80:81], v18 offset:12288\n"
    "v_mfma_f32_16x16x32_bf16 v[112:115], a[32:35], v[96:99], v[112:115]\n"
    "ds_read_b64_tr_b16 v[82:83], v18 offset:14336\n"
    "v_mfma_f32_16x16x32_bf16 v[128:131], a[36:39], v[96:99], v[128:131]\n"
    "ds_read_b64_tr_b16 v[84:85], v19 offset:12288\n"
    "v_mfma_f32_16x16x32_bf16 v[144:147], a[40:43], v[96:99], v[144:147]\n"
    "ds_read_b64_tr_b16 v[86:87], v19 offset:14336\n"
    "v_mfma_f32_16x16x32_bf16 v[160:163], a[44:47], v[96:99], v[160:163]\n"
    "ds_read_b64_tr_b16 v[88:89], v20 offset:12288\n"
    "s_waitcnt lgkmcnt(9)\n"
    "v_mfma_f32_16x16x32_bf16 v[116:119], a[32:35], v[100:103], v[116:119]\n"
    "ds_read_b64_tr_b16 v[90:91], v20 offset:14336\n"
    "v_mfma_f32_16x16x32_bf16 v[132:135], a[36:39], v[100:103], v[132:135]\n"
    "ds_read_b64_tr_b16 v[92:93], v21 offset:12288\n"
    "v_mfma_f32_16x16x32_bf16 v[148:151], a[40:43], v[100:103], v[148:151]\n"
    "ds_read_b64_tr_b16 v[94:95], v21 offset:14336\n"
    "v_mfma_f32_16x16x32_bf16 v[164:167], a[44:47], v[100:103], v[164:167]\n"
    "s_mov_b32 s21, 14188544\n"
    "buffer_load_dwordx4 a[16:19], v51, s[32:35], s21 offen offset:0 sc1\n"
    "s_waitcnt lgkmcnt(10)\n"
    "v_mfma_f32_16x16x32_bf16 v[120:123], a[32:35], v[104:107], v[120:123]\n"
    "v_mfma_f32_16x16x32_bf16 v[136:139], a[36:39], v[104:107], v[136:139]\n"
    "buffer_load_dwordx4 a[20:23], v51, s[32:35], s21 offen offset:1024 sc1\n"
    "v_mfma_f32_16x16x32_bf16 v[152:155], a[40:43], v[104:107], v[152:155]\n"
    "v_mfma_f32_16x16x32_bf16 v[72:75], a[44:47], v[104:107], v[72:75]\n"
    "buffer_load_dwordx4 a[24:27], v52, s[32:35], s21 offen offset:0 sc1\n"
    "s_waitcnt lgkmcnt(8)\n"
    "v_mfma_f32_16x16x32_bf16 v[124:127], a[32:35], v[108:111], v[124:127]\n"
    "v_mfma_f32_16x16x32_bf16 v[140:143], a[36:39], v[108:111], v[140:143]\n"
    "buffer_load_dwordx4 a[28:31], v52, s[32:35], s21 offen offset:1024 sc1\n"
    "v_mfma_f32_16x16x32_bf16 v[156:159], a[40:43], v[108:111], v[156:159]\n"
    "v_mfma_f32_16x16x32_bf16 v[76:79], a[44:47], v[108:111], v[76:79]\n"
    "s_waitcnt vmcnt(19)\n"
    "s_barrier\n"
    "s_waitcnt lgkmcnt(6)\n"
    "v_mfma_f32_16x16x32_bf16 v[6:9], v[58:61], v[80:83], v[6:9]\n"
    "ds_read_b64_tr_b16 v[96:97], v18 offset:16384\n"
    "v_mfma_f32_16x16x32_bf16 v[112:115], a[48:51], v[80:83], v[112:115]\n"
    "ds_read_b64_tr_b16 v[98:99], v18 offset:18432\n"
    "v_mfma_f32_16x16x32_bf16 v[128:131], a[52:55], v[80:83], v[128:131]\n"
    "ds_read_b64_tr_b16 v[100:101], v19 offset:16384\n"
    "v_mfma_f32_16x16x32_bf16 v[144:147], a[56:59], v[80:83], v[144:147]\n"
    "ds_read_b64_tr_b16 v[102:103], v19 offset:18432\n"
    "v_mfma_f32_16x16x32_bf16 v[160:163], a[60:63], v[80:83], v[160:163]\n"
    "ds_read_b64_tr_b16 v[104:105], v20 offset:16384\n"
    "s_waitcnt lgkmcnt(9)\n"
    "v_mfma_f32_16x16x32_bf16 v[116:119], a[48:51], v[84:87], v[116:119]\n"
    "ds_read_b64_tr_b16 v[106:107], v20 offset:18432\n"
    "v_mfma_f32_16x16x32_bf16 v[132:135], a[52:55], v[84:87], v[132:135]\n"
    "ds_read_b64_tr_b16 v[108:109], v21 offset:16384\n"
    "v_mfma_f32_16x16x32_bf16 v[148:151], a[56:59], v[84:87], v[148:151]\n"
    "ds_read_b64_tr_b16 v[110:111], v21 offset:18432\n"
    "v_mfma_f32_16x16x32_bf16 v[164:167], a[60:63], v[84:87], v[164:167]\n"
    "s_mov_b32 s21, 14221312\n"
    "buffer_load_dwordx4 a[32:35], v51, s[32:35], s21 offen offset:0 sc1\n"
    "s_waitcnt lgkmcnt(10)\n"
    "v_mfma_f32_16x16x32_bf16 v[120:123], a[48:51], v[88:91], v[120:123]\n"
    "v_mfma_f32_16x16x32_bf16 v[136:139], a[52:55], v[88:91], v[136:139]\n"
    "buffer_load_dwordx4 a[36:39], v51, s[32:35], s21 offen offset:1024 sc1\n"
    "v_mfma_f32_16x16x32_bf16 v[152:155], a[56:59], v[88:91], v[152:155]\n"
    "v_mfma_f32_16x16x32_bf16 v[72:75], a[60:63], v[88:91], v[72:75]\n"
    "buffer_load_dwordx4 a[40:43], v52, s[32:35], s21 offen offset:0 sc1\n"
    "s_waitcnt lgkmcnt(8)\n"
    "v_mfma_f32_16x16x32_bf16 v[124:127], a[48:51], v[92:95], v[124:127]\n"
    "v_mfma_f32_16x16x32_bf16 v[140:143], a[52:55], v[92:95], v[140:143]\n"
    "buffer_load_dwordx4 a[44:47], v52, s[32:35], s21 offen offset:1024 sc1\n"
    "v_mfma_f32_16x16x32_bf16 v[156:159], a[56:59], v[92:95], v[156:159]\n"
    "v_mfma_f32_16x16x32_bf16 v[76:79], a[60:63], v[92:95], v[76:79]\n"
    "s_waitcnt vmcnt(12)\n"
    "s_barrier\n"
    "s_add_u32 s67, s18, 1441792\n"
    "s_mov_b64 exec, 1\n"
    "buffer_store_dwordx2 v[62:63], v50, s[32:35], s67 offen sc1\n"
    "s_mov_b64 exec, -1\n"
    "s_waitcnt lgkmcnt(6)\n"
    "v_mfma_f32_16x16x32_bf16 v[6:9], v[58:61], v[96:99], v[6:9]\n"
    "ds_read_b64_tr_b16 v[80:81], v18 offset:20480\n"
    "v_mfma_f32_16x16x32_bf16 v[112:115], a[64:67], v[96:99], v[112:115]\n"
    "ds_read_b64_tr_b16 v[82:83], v18 offset:22528\n"
    "v_mfma_f32_16x16x32_bf16 v[128:131], a[68:71], v[96:99], v[128:131]\n"
    "ds_read_b64_tr_b16 v[84:85], v19 offset:20480\n"
    "v_mfma_f32_16x16x32_bf16 v[144:147], a[72:75], v[96:99], v[144:147]\n"
    "ds_read_b64_tr_b16 v[86:87], v19 offset:22528\n"
    "v_mfma_f32_16x16x32_bf16 v[160:163], a[76:79], v[96:99], v[160:163]\n"
    "ds_read_b64_tr_b16 v[88:89], v20 offset:20480\n"
    "s_waitcnt lgkmcnt(9)\n"
    "v_mfma_f32_16x16x32_bf16 v[116:119], a[64:67], v[100:103], v[116:119]\n"
    "ds_read_b64_tr_b16 v[90:91], v20 offset:22528\n"
    "v_mfma_f32_16x16x32_bf16 v[132:135], a[68:71], v[100:103], v[132:135]\n"
    "ds_read_b64_tr_b16 v[92:93], v21 offset:20480\n"
    "v_mfma_f32_16x16x32_bf16 v[148:151], a[72:75], v[100:103], v[148:151]\n"
    "ds_read_b64_tr_b16 v[94:95], v21 offset:22528\n"
    "v_mfma_f32_16x16x32_bf16 v[164:167], a[76:79], v[100:103], v[164:167]\n"
    "s_mov_b32 s21, 14254080\n"
    "buffer_load_dwordx4 a[48:51], v51, s[32:35], s21 offen offset:0 sc1\n"
    "s_waitcnt lgkmcnt(10)\n"
    "v_mfma_f32_16x16x32_bf16 v[120:123], a[64:67], v[104:107], v[120:123]\n"
    "v_mfma_f32_16x16x32_bf16 v[136:139], a[68:71], v[104:107], v[136:139]\n"
    "buffer_load_dwordx4 a[52:55], v51, s[32:35], s21 offen offset:1024 sc1\n"
    "v_mfma_f32_16x16x32_bf16 v[152:155], a[72:75], v[104:107], v[152:155]\n"
    "v_mfma_f32_16x16x32_bf16 v[72:75], a[76:79], v[104:107], v[72:75]\n"
    "buffer_load_dwordx4 a[56:59], v52, s[32:35], s21 offen offset:0 sc1\n"
    "s_waitcnt lgkmcnt(8)\n"
    "v_mfma_f32_16x16x32_bf16 v[124:127], a[64:67], v[108:111], v[124:127]\n"
    "v_mfma_f32_16x16x32_bf16 v[140:143], a[68:71], v[108:111], v[140:143]\n"
    "buffer_load_dwordx4 a[60:63], v52, s[32:35], s21 offen offset:1024 sc1\n"
    "v_mfma_f32_16x16x32_bf16 v[156:159], a[72:75], v[108:111], v[156:159]\n"
    "v_mfma_f32_16x16x32_bf16 v[76:79], a[76:79], v[108:111], v[76:79]\n"
    "s_waitcnt lgkmcnt(6)\n"
    "v_mfma_f32_16x16x32_bf16 v[6:9], v[58:61], v[80:83], v[6:9]\n"
    "ds_read_b64_tr_b16 v[96:97], v18 offset:24576\n"
    "v_mfma_f32_16x16x32_bf16 v[112:115], a[80:83], v[80:83], v[112:115]\n"
    "ds_read_b64_tr_b16 v[98:99], v18 offset:26624\n"
    "v_mfma_f32_16x16x32_bf16 v[128:131], a[84:87], v[80:83], v[128:131]\n"
    "ds_read_b64_tr_b16 v[100:101], v19 offset:24576\n"
    "v_mfma_f32_16x16x32_bf16 v[144:147], a[88:91], v[80:83], v[144:147]\n"
    "ds_read_b64_tr_b16 v[102:103], v19 offset:26624\n"
    "v_mfma_f32_16x16x32_bf16 v[160:163], a[92:95], v[80:83], v[160:163]\n"
    "ds_read_b64_tr_b16 v[104:105], v20 offset:24576\n"
    "s_waitcnt lgkmcnt(9)\n"
    "v_mfma_f32_16x16x32_bf16 v[116:119], a[80:83], v[84:87], v[116:119]\n"
    "ds_read_b64_tr_b16 v[106:107], v20 offset:26624\n"
    "v_mfma_f32_16x16x32_bf16 v[132:135], a[84:87], v[84:87], v[132:135]\n"
    "ds_read_b64_tr_b16 v[108:109], v21 offset:24576\n"
    "v_mfma_f32_16x16x32_bf16 v[148:151], a[88:91], v[84:87], v[148:151]\n"
    "ds_read_b64_tr_b16 v[110:111], v21 offset:26624\n"
    "v_mfma_f32_16x16x32_bf16 v[164:167], a[92:95], v[84:87], v[164:167]\n"
    "s_mov_b32 s21, 14286848\n"
    "buffer_load_dwordx4 a[64:67], v51, s[32:35], s21 offen offset:0 sc1\n"
    "s_waitcnt lgkmcnt(10)\n"
    "v_mfma_f32_16x16x32_bf16 v[120:123], a[80:83], v[88:91], v[120:123]\n"
    "v_mfma_f32_16x16x32_bf16 v[136:139], a[84:87], v[88:91], v[136:139]\n"
    "buffer_load_dwordx4 a[68:71], v51, s[32:35], s21 offen offset:1024 sc1\n"
    "v_mfma_f32_16x16x32_bf16 v[152:155], a[88:91], v[88:91], v[152:155]\n"
    "v_mfma_f32_16x16x32_bf16 v[72:75], a[92:95], v[88:91], v[72:75]\n"
    "buffer_load_dwordx4 a[72:75], v52, s[32:35], s21 offen offset:0 sc1\n"
    "s_waitcnt lgkmcnt(8)\n"
    "v_mfma_f32_16x16x32_bf16 v[124:127], a[80:83], v[92:95], v[124:127]\n"
    "v_mfma_f32_16x16x32_bf16 v[140:143], a[84:87], v[92:95], v[140:143]\n"
    "buffer_load_dwordx4 a[76:79], v52, s[32:35], s21 offen offset:1024 sc1\n"
    "v_mfma_f32_16x16x32_bf16 v[156:159], a[88:91], v[92:95], v[156:159]\n"
    "v_mfma_f32_16x16x32_bf16 v[76:79], a[92:95], v[92:95], v[76:79]\n"
    "s_waitcnt lgkmcnt(6)\n"
    "v_mfma_f32_16x16x32_bf16 v[6:9], v[58:61], v[96:99], v[6:9]\n"
    "ds_read_b64_tr_b16 v[80:81], v18 offset:28672\n"
    "v_mfma_f32_16x16x32_bf16 v[112:115], a[96:99], v[96:99], v[112:115]\n"
    "ds_read_b64_tr_b16 v[82:83], v18 offset:30720\n"
    "v_mfma_f32_16x16x32_bf16 v[128:131], a[100:103], v[96:99], v[128:131]\n"
    "ds_read_b64_tr_b16 v[84:85], v19 offset:28672\n"
    "v_mfma_f32_16x16x32_bf16 v[144:147], a[104:107], v[96:99], v[144:147]\n"
    "ds_read_b64_tr_b16 v[86:87], v19 offset:30720\n"
    "v_mfma_f32_16x16x32_bf16 v[160:163], a[108:111], v[96:99], v[160:163]\n"
    "ds_read_b64_tr_b16 v[88:89], v20 offset:28672\n"
    "s_waitcnt lgkmcnt(9)\n"
    "v_mfma_f32_16x16x32_bf16 v[116:119], a[96:99], v[100:103], v[116:119]\n"
    "ds_read_b64_tr_b16 v[90:91], v20 offset:30720\n"
    "v_mfma_f32_16x16x32_bf16 v[132:135], a[100:103], v[100:103], v[132:135]\n"
    "ds_read_b64_tr_b16 v[92:93], v21 offset:28672\n"
    "v_mfma_f32_16x16x32_bf16 v[148:151], a[104:107], v[100:103], v[148:151]\n"
    "ds_read_b64_tr_b16 v[94:95], v21 offset:30720\n"
    "v_mfma_f32_16x16x32_bf16 v[164:167], a[108:111], v[100:103], v[164:167]\n"
    "s_mov_b32 s21, 14319616\n"
    "buffer_load_dwordx4 a[80:83], v51, s[32:35], s21 offen offset:0 sc1\n"
    "s_waitcnt lgkmcnt(10)\n"
    "v_mfma_f32_16x16x32_bf16 v[120:123], a[96:99], v[104:107], v[120:123]\n"
    "v_mfma_f32_16x16x32_bf16 v[136:139], a[100:103], v[104:107], v[136:139]\n"
    "buffer_load_dwordx4 a[84:87], v51, s[32:35], s21 offen offset:1024 sc1\n"
    "v_mfma_f32_16x16x32_bf16 v[152:155], a[104:107], v[104:107], v[152:155]\n"
    "v_mfma_f32_16x16x32_bf16 v[72:75], a[108:111], v[104:107], v[72:75]\n"
    "buffer_load_dwordx4 a[88:91], v52, s[32:35], s21 offen offset:0 sc1\n"
    "s_waitcnt lgkmcnt(8)\n"
    "v_mfma_f32_16x16x32_bf16 v[124:127], a[96:99], v[108:111], v[124:127]\n"
    "v_mfma_f32_16x16x32_bf16 v[140:143], a[100:103], v[108:111], v[140:143]\n"
    "buffer_load_dwordx4 a[92:95], v52, s[32:35], s21 offen offset:1024 sc1\n"
    "v_mfma_f32_16x16x32_bf16 v[156:159], a[104:107], v[108:111], v[156:159]\n"
    "v_mfma_f32_16x16x32_bf16 v[76:79], a[108:111], v[108:111], v[76:79]\n"
    "v_subrev_u32 v18, s48, v18\n"
    "v_subrev_u32 v19, s48, v19\n"
    "v_subrev_u32 v20, s48, v20\n"
    "v_subrev_u32 v21, s48, v21\n"
    "s_waitcnt lgkmcnt(6)\n"
    "v_mfma_f32_16x16x32_bf16 v[6:9], v[58:61], v[80:83], v[6:9]\n"
    "ds_read_b64_tr_b16 v[96:97], v18 offset:32768\n"
    "v_mfma_f32_16x16x32_bf16 v[112:115], a[112:115], v[80:83], v[112:115]\n"
    "ds_read_b64_tr_b16 v[98:99], v18 offset:34816\n"
    "v_mfma_f32_16x16x32_bf16 v[128:131], a[116:119], v[80:83], v[128:131]\n"
    "ds_read_b64_tr_b16 v[100:101], v19 offset:32768\n"
    "v_mfma_f32_16x16x32_bf16 v[144:147], a[120:123], v[80:83], v[144:147]\n"
    "ds_read_b64_tr_b16 v[102:103], v19 offset:34816\n"
    "v_mfma_f32_16x16x32_bf16 v[160:163], a[124:127], v[80:83], v[160:163]\n"
    "ds_read_b64_tr_b16 v[104:105], v20 offset:32768\n"
    "s_waitcnt lgkmcnt(9)\n"
    "v_mfma_f32_16x16x32_bf16 v[116:119], a[112:115], v[84:87], v[116:119]\n"
    "ds_read_b64_tr_b16 v[106:107], v20 offset:34816\n"
    "v_mfma_f32_16x16x32_bf16 v[132:135], a[116:119], v[84:87], v[132:135]\n"
    "ds_read_b64_tr_b16 v[108:109], v21 offset:32768\n"
    "v_mfma_f32_16x16x32_bf16 v[148:151], a[120:123], v[84:87], v[148:151]\n"
    "ds_read_b64_tr_b16 v[110:111], v21 offset:34816\n"
    "v_mfma_f32_16x16x32_bf16 v[164:167], a[124:127], v[84:87], v[164:167]\n"
    "s_mov_b32 s21, 14352384\n"
    "buffer_load_dwordx4 a[96:99], v51, s[32:35], s21 offen offset:0 sc1\n"
    "s_waitcnt lgkmcnt(10)\n"
    "v_mfma_f32_16x16x32_bf16 v[120:123], a[112:115], v[88:91], v[120:123]\n"
    "v_mfma_f32_16x16x32_bf16 v[136:139], a[116:119], v[88:91], v[136:139]\n"
    "buffer_load_dwordx4 a[100:103], v51, s[32:35], s21 offen offset:1024 sc1\n"
    "v_mfma_f32_16x16x32_bf16 v[152:155], a[120:123], v[88:91], v[152:155]\n"
    "v_mfma_f32_16x16x32_bf16 v[72:75], a[124:127], v[88:91], v[72:75]\n"
    "buffer_load_dwordx4 a[104:107], v52, s[32:35], s21 offen offset:0 sc1\n"
    "s_waitcnt lgkmcnt(8)\n"
    "v_mfma_f32_16x16x32_bf16 v[124:127], a[112:115], v[92:95], v[124:127]\n"
    "v_mfma_f32_16x16x32_bf16 v[140:143], a[116:119], v[92:95], v[140:143]\n"
    "buffer_load_dwordx4 a[108:111], v52, s[32:35], s21 offen offset:1024 sc1\n"
    "v_mfma_f32_16x16x32_bf16 v[156:159], a[120:123], v[92:95], v[156:159]\n"
    "v_mfma_f32_16x16x32_bf16 v[76:79], a[124:127], v[92:95], v[76:79]\n"
    "s_waitcnt lgkmcnt(6)\n"
    "v_mfma_f32_16x16x32_bf16 v[6:9], v[58:61], v[96:99], v[6:9]\n"
    "ds_read_b64_tr_b16 v[80:81], v18 offset:36864\n"
    "v_mfma_f32_16x16x32_bf16 v[112:115], a[128:131], v[96:99], v[112:115]\n"
    "ds_read_b64_tr_b16 v[82:83], v18 offset:38912\n"
    "v_mfma_f32_16x16x32_bf16 v[128:131], a[132:135], v[96:99], v[128:131]\n"
    "ds_read_b64_tr_b16 v[84:85], v19 offset:36864\n"
    "v_mfma_f32_16x16x32_bf16 v[144:147], a[136:139], v[96:99], v[144:147]\n"
    "ds_read_b64_tr_b16 v[86:87], v19 offset:38912\n"
    "s_waitcnt lgkmcnt(8)\n"
    "v_mfma_f32_16x16x32_bf16 v[116:119], a[128:131], v[100:103], v[116:119]\n"
    "ds_read_b64_tr_b16 v[88:89], v20 offset:36864\n"
    "v_mfma_f32_16x16x32_bf16 v[132:135], a[132:135], v[100:103], v[132:135]\n"
    "ds_read_b64_tr_b16 v[90:91], v20 offset:38912\n"
    "v_mfma_f32_16x16x32_bf16 v[148:151], a[136:139], v[100:103], v[148:151]\n"
    "ds_read_b64_tr_b16 v[92:93], v21 offset:36864\n"
    "s_waitcnt lgkmcnt(9)\n"
    "v_mfma_f32_16x16x32_bf16 v[120:123], a[128:131], v[104:107], v[120:123]\n"
    "ds_read_b64_tr_b16 v[94:95], v21 offset:38912\n"
    "v_mfma_f32_16x16x32_bf16 v[136:139], a[132:135], v[104:107], v[136:139]\n"
    "s_mov_b32 s21, 14385152\n"
    "buffer_load_dwordx4 a[112:115], v51, s[32:35], s21 offen offset:0 sc1\n"
    "v_mfma_f32_16x16x32_bf16 v[152:155], a[136:139], v[104:107], v[152:155]\n"
    "buffer_load_dwordx4 a[116:119], v51, s[32:35], s21 offen offset:1024 sc1\n"
    "s_waitcnt lgkmcnt(8)\n"
    "v_mfma_f32_16x16x32_bf16 v[124:127], a[128:131], v[108:111], v[124:127]\n"
    "v_mfma_f32_16x16x32_bf16 v[140:143], a[132:135], v[108:111], v[140:143]\n"
    "buffer_load_dwordx4 a[120:123], v52, s[32:35], s21 offen offset:0 sc1\n"
    "v_mfma_f32_16x16x32_bf16 v[156:159], a[136:139], v[108:111], v[156:159]\n"
    "buffer_load_dwordx4 a[124:127], v52, s[32:35], s21 offen offset:1024 sc1\n"
    "s_waitcnt lgkmcnt(6)\n"
    "v_mfma_f32_16x16x32_bf16 v[6:9], v[58:61], v[80:83], v[6:9]\n"
    "ds_read_b64_tr_b16 v[96:97], v18 offset:40960\n"
    "v_mfma_f32_16x16x32_bf16 v[112:115], a[144:147], v[80:83], v[112:115]\n"
    "ds_read_b64_tr_b16 v[98:99], v18 offset:43008\n"
    "v_mfma_f32_16x16x32_bf16 v[128:131], a[148:151], v[80:83], v[128:131]\n"
    "ds_read_b64_tr_b16 v[100:101], v19 offset:40960\n"
    "v_mfma_f32_16x16x32_bf16 v[144:147], a[152:155], v[80:83], v[144:147]\n"
    "ds_read_b64_tr_b16 v[102:103], v19 offset:43008\n"
    "s_waitcnt lgkmcnt(8)\n"
    "v_mfma_f32_16x16x32_bf16 v[116:119], a[144:147], v[84:87], v[116:119]\n"
    "ds_read_b64_tr_b16 v[104:105], v20 offset:40960\n"
    "v_mfma_f32_16x16x32_bf16 v[132:135], a[148:151], v[84:87], v[132:135]\n"
    "ds_read_b64_tr_b16 v[106:107], v20 offset:43008\n"
    "v_mfma_f32_16x16x32_bf16 v[148:151], a[152:155], v[84:87], v[148:151]\n"
    "ds_read_b64_tr_b16 v[108:109], v21 offset:40960\n"
    "s_waitcnt lgkmcnt(9)\n"
    "v_mfma_f32_16x16x32_bf16 v[120:123], a[144:147], v[88:91], v[120:123]\n"
    "ds_read_b64_tr_b16 v[110:111], v21 offset:43008\n"
    "v_mfma_f32_16x16x32_bf16 v[136:139], a[148:151], v[88:91], v[136:139]\n"
    "s_mov_b32 s21, 14417920\n"
    "buffer_load_dwordx4 a[128:131], v51, s[32:35], s21 offen offset:0 sc1\n"
    "v_mfma_f32_16x16x32_bf16 v[152:155], a[152:155], v[88:91], v[152:155]\n"
    "buffer_load_dwordx4 a[132:135], v51, s[32:35], s21 offen offset:1024 sc1\n"
    "ds_write_b128 v250, v[72:75] offset:0\n"
    "s_waitcnt lgkmcnt(9)\n"
    "v_mfma_f32_16x16x32_bf16 v[124:127], a[144:147], v[92:95], v[124:127]\n"
    "v_mfma_f32_16x16x32_bf16 v[140:143], a[148:151], v[92:95], v[140:143]\n"
    "buffer_load_dwordx4 a[136:139], v52, s[32:35], s21 offen offset:0 sc1\n"
    "ds_write_b128 v250, v[76:79] offset:1024\n"
    "v_mfma_f32_16x16x32_bf16 v[156:159], a[152:155], v[92:95], v[156:159]\n"
    "s_waitcnt lgkmcnt(8)\n"
    "v_mfma_f32_16x16x32_bf16 v[6:9], v[58:61], v[96:99], v[6:9]\n"
    "ds_read_b64_tr_b16 v[80:81], v18 offset:45056\n"
    "v_mfma_f32_16x16x32_bf16 v[112:115], a[160:163], v[96:99], v[112:115]\n"
    "ds_read_b64_tr_b16 v[82:83], v18 offset:47104\n"
    "v_mfma_f32_16x16x32_bf16 v[128:131], a[164:167], v[96:99], v[128:131]\n"
    "ds_read_b64_tr_b16 v[84:85], v19 offset:45056\n"
    "v_mfma_f32_16x16x32_bf16 v[144:147], a[168:171], v[96:99], v[144:147]\n"
    "ds_read_b64_tr_b16 v[86:87], v19 offset:47104\n"
    "s_waitcnt lgkmcnt(10)\n"
    "v_mfma_f32_16x16x32_bf16 v[116:119], a[160:163], v[100:103], v[116:119]\n"
    "ds_read_b64_tr_b16 v[88:89], v20 offset:45056\n"
    "v_mfma_f32_16x16x32_bf16 v[132:135], a[164:167], v[100:103], v[132:135]\n"
    "ds_read_b64_tr_b16 v[90:91], v20 offset:47104\n"
    "v_mfma_f32_16x16x32_bf16 v[148:151], a[168:171], v[100:103], v[148:151]\n"
    "ds_read_b64_tr_b16 v[92:93], v21 offset:45056\n"
    "s_waitcnt lgkmcnt(11)\n"
    "v_mfma_f32_16x16x32_bf16 v[120:123], a[160:163], v[104:107], v[120:123]\n"
    "ds_read_b64_tr_b16 v[94:95], v21 offset:47104\n"
    "v_mfma_f32_16x16x32_bf16 v[136:139], a[164:167], v[104:107], v[136:139]\n"
    "s_mov_b32 s21, 14450688\n"
    "buffer_load_dwordx4 a[144:147], v51, s[32:35], s21 offen offset:0 sc1\n"
    "v_mfma_f32_16x16x32_bf16 v[152:155], a[168:171], v[104:107], v[152:155]\n"
    "buffer_load_dwordx4 a[148:151], v51, s[32:35], s21 offen offset:1024 sc1\n"
    "s_waitcnt lgkmcnt(10)\n"
    "v_mfma_f32_16x16x32_bf16 v[124:127], a[160:163], v[108:111], v[124:127]\n"
    "v_mfma_f32_16x16x32_bf16 v[140:143], a[164:167], v[108:111], v[140:143]\n"
    "buffer_load_dwordx4 a[152:155], v52, s[32:35], s21 offen offset:0 sc1\n"
    "v_mfma_f32_16x16x32_bf16 v[156:159], a[168:171], v[108:111], v[156:159]\n"
    "s_add_u32 s4, s20, 1441792\n"
    "buffer_load_dwordx2 v[14:15], v55, s[32:35], s4 offen sc1\n"
    "s_waitcnt lgkmcnt(6)\n"
    "v_mfma_f32_16x16x32_bf16 v[6:9], v[58:61], v[80:83], v[6:9]\n"
    "ds_read_b64_tr_b16 v[96:97], v18 offset:49152\n"
    "v_mfma_f32_16x16x32_bf16 v[112:115], a[176:179], v[80:83], v[112:115]\n"
    "ds_read_b64_tr_b16 v[98:99], v18 offset:51200\n"
    "v_mfma_f32_16x16x32_bf16 v[128:131], a[180:183], v[80:83], v[128:131]\n"
    "ds_read_b64_tr_b16 v[100:101], v19 offset:49152\n"
    "v_mfma_f32_16x16x32_bf16 v[144:147], a[184:187], v[80:83], v[144:147]\n"
    "ds_read_b64_tr_b16 v[102:103], v19 offset:51200\n"
    "s_waitcnt lgkmcnt(8)\n"
    "v_mfma_f32_16x16x32_bf16 v[116:119], a[176:179], v[84:87], v[116:119]\n"
    "ds_read_b64_tr_b16 v[104:105], v20 offset:49152\n"
    "v_mfma_f32_16x16x32_bf16 v[132:135], a[180:183], v[84:87], v[132:135]\n"
    "ds_read_b64_tr_b16 v[106:107], v20 offset:51200\n"
    "v_mfma_f32_16x16x32_bf16 v[148:151], a[184:187], v[84:87], v[148:151]\n"
    "ds_read_b64_tr_b16 v[108:109], v21 offset:49152\n"
    "s_waitcnt lgkmcnt(9)\n"
    "v_mfma_f32_16x16x32_bf16 v[120:123], a[176:179], v[88:91], v[120:123]\n"
    "ds_read_b64_tr_b16 v[110:111], v21 offset:51200\n"
    "v_mfma_f32_16x16x32_bf16 v[136:139], a[180:183], v[88:91], v[136:139]\n"
    "s_mov_b32 s21, 14483456\n"
    "buffer_load_dwordx4 a[160:163], v51, s[32:35], s21 offen offset:0 sc1\n"
    "v_mfma_f32_16x16x32_bf16 v[152:155], a[184:187], v[88:91], v[152:155]\n"
    "buffer_load_dwordx4 a[164:167], v51, s[32:35], s21 offen offset:1024 sc1\n"
    "ds_read_b64_tr_b16 v[30:31], v10\n"
    "s_waitcnt lgkmcnt(9)\n"
    "v_mfma_f32_16x16x32_bf16 v[124:127], a[176:179], v[92:95], v[124:127]\n"
    "v_mfma_f32_16x16x32_bf16 v[140:143], a[180:183], v[92:95], v[140:143]\n"
    "buffer_load_dwordx4 a[168:171], v52, s[32:35], s21 offen offset:0 sc1\n"
    "ds_read_b64_tr_b16 v[32:33], v10 offset:2048\n"
    "v_mfma_f32_16x16x32_bf16 v[156:159], a[184:187], v[92:95], v[156:159]\n"
    "s_waitcnt lgkmcnt(8)\n"
    "v_mfma_f32_16x16x32_bf16 v[6:9], v[58:61], v[96:99], v[6:9]\n"
    "ds_read_b64_tr_b16 v[80:81], v18 offset:53248\n"
    "v_mfma_f32_16x16x32_bf16 v[112:115], a[192:195], v[96:99], v[112:115]\n"
    "ds_read_b64_tr_b16 v[82:83], v18 offset:55296\n"
    "v_mfma_f32_16x16x32_bf16 v[128:131], a[196:199], v[96:99], v[128:131]\n"
    "ds_read_b64_tr_b16 v[84:85], v19 offset:53248\n"
    "v_mfma_f32_16x16x32_bf16 v[144:147], a[200:203], v[96:99], v[144:147]\n"
    "ds_read_b64_tr_b16 v[86:87], v19 offset:55296\n"
    "s_waitcnt lgkmcnt(10)\n"
    "v_mfma_f32_16x16x32_bf16 v[116:119], a[192:195], v[100:103], v[116:119]\n"
    "ds_read_b64_tr_b16 v[88:89], v20 offset:53248\n"
    "v_mfma_f32_16x16x32_bf16 v[132:135], a[196:199], v[100:103], v[132:135]\n"
    "ds_read_b64_tr_b16 v[90:91], v20 offset:55296\n"
    "v_mfma_f32_16x16x32_bf16 v[148:151], a[200:203], v[100:103], v[148:151]\n"
    "ds_read_b64_tr_b16 v[92:93], v21 offset:53248\n"
    "s_waitcnt lgkmcnt(11)\n"
    "v_mfma_f32_16x16x32_bf16 v[120:123], a[192:195], v[104:107], v[120:123]\n"
    "ds_read_b64_tr_b16 v[94:95], v21 offset:55296\n"
    "v_mfma_f32_16x16x32_bf16 v[136:139], a[196:199], v[104:107], v[136:139]\n"
    "s_mov_b32 s21, 14516224\n"
    "buffer_load_dwordx4 a[176:179], v51, s[32:35], s21 offen offset:0 sc1\n"
    "v_mfma_f32_16x16x32_bf16 v[152:155], a[200:203], v[104:107], v[152:155]\n"
    "buffer_load_dwordx4 a[180:183], v51, s[32:35], s21 offen offset:1024 sc1\n"
    "s_waitcnt lgkmcnt(10)\n"
    "v_mfma_f32_16x16x32_bf16 v[124:127], a[192:195], v[108:111], v[124:127]\n"
    "v_mfma_f32_16x16x32_bf16 v[140:143], a[196:199], v[108:111], v[140:143]\n"
    "buffer_load_dwordx4 a[184:187], v52, s[32:35], s21 offen offset:0 sc1\n"
    "v_mfma_f32_16x16x32_bf16 v[156:159], a[200:203], v[108:111], v[156:159]\n"
    "s_waitcnt lgkmcnt(8)\n"
    "v_mfma_f32_16x16x32_bf16 v[252:255], v[30:33], v[30:33], 0\n"
    "s_waitcnt lgkmcnt(6)\n"
    "v_mfma_f32_16x16x32_bf16 v[6:9], v[58:61], v[80:83], v[6:9]\n"
    "ds_read_b64_tr_b16 v[96:97], v18 offset:57344\n"
    "v_mfma_f32_16x16x32_bf16 v[112:115], a[208:211], v[80:83], v[112:115]\n"
    "ds_read_b64_tr_b16 v[98:99], v18 offset:59392\n"
    "v_mfma_f32_16x16x32_bf16 v[128:131], a[212:215], v[80:83], v[128:131]\n"
    "ds_read_b64_tr_b16 v[100:101], v19 offset:57344\n"
    "v_mfma_f32_16x16x32_bf16 v[144:147], a[216:219], v[80:83], v[144:147]\n"
    "ds_read_b64_tr_b16 v[102:103], v19 offset:59392\n"
    "s_waitcnt lgkmcnt(8)\n"
    "v_mfma_f32_16x16x32_bf16 v[116:119], a[208:211], v[84:87], v[116:119]\n"
    "ds_read_b64_tr_b16 v[104:105], v20 offset:57344\n"
    "v_mfma_f32_16x16x32_bf16 v[132:135], a[212:215], v[84:87], v[132:135]\n"
    "ds_read_b64_tr_b16 v[106:107], v20 offset:59392\n"
    "v_mfma_f32_16x16x32_bf16 v[148:151], a[216:219], v[84:87], v[148:151]\n"
    "ds_read_b64_tr_b16 v[108:109], v21 offset:57344\n"
    "s_waitcnt lgkmcnt(9)\n"
    "v_mfma_f32_16x16x32_bf16 v[120:123], a[208:211], v[88:91], v[120:123]\n"
    "ds_read_b64_tr_b16 v[110:111], v21 offset:59392\n"
    "v_mfma_f32_16x16x32_bf16 v[136:139], a[212:215], v[88:91], v[136:139]\n"
    "s_mov_b32 s21, 14548992\n"
    "buffer_load_dwordx4 a[192:195], v51, s[32:35], s21 offen offset:0 sc1\n"
    "v_mfma_f32_16x16x32_bf16 v[152:155], a[216:219], v[88:91], v[152:155]\n"
    "buffer_load_dwordx4 a[196:199], v51, s[32:35], s21 offen offset:1024 sc1\n"
    "s_waitcnt lgkmcnt(8)\n"
    "v_mfma_f32_16x16x32_bf16 v[124:127], a[208:211], v[92:95], v[124:127]\n"
    "v_mfma_f32_16x16x32_bf16 v[140:143], a[212:215], v[92:95], v[140:143]\n"
    "buffer_load_dwordx4 a[200:203], v52, s[32:35], s21 offen offset:0 sc1\n"
    "v_mfma_f32_16x16x32_bf16 v[156:159], a[216:219], v[92:95], v[156:159]\n"
    "s_waitcnt lgkmcnt(6)\n"
    "v_mfma_f32_16x16x32_bf16 v[6:9], v[58:61], v[96:99], v[6:9]\n"
    "ds_read_b64_tr_b16 v[80:81], v18 offset:61440\n"
    "v_mfma_f32_16x16x32_bf16 v[112:115], a[224:227], v[96:99], v[112:115]\n"
    "ds_read_b64_tr_b16 v[82:83], v18 offset:63488\n"
    "v_mfma_f32_16x16x32_bf16 v[128:131], a[228:231], v[96:99], v[128:131]\n"
    "ds_read_b64_tr_b16 v[84:85], v19 offset:61440\n"
    "v_mfma_f32_16x16x32_bf16 v[144:147], a[232:235], v[96:99], v[144:147]\n"
    "ds_read_b64_tr_b16 v[86:87], v19 offset:63488\n"
    "s_waitcnt lgkmcnt(8)\n"
    "v_mfma_f32_16x16x32_bf16 v[116:119], a[224:227], v[100:103], v[116:119]\n"
    "ds_read_b64_tr_b16 v[88:89], v20 offset:61440\n"
    "v_mfma_f32_16x16x32_bf16 v[132:135], a[228:231], v[100:103], v[132:135]\n"
    "ds_read_b64_tr_b16 v[90:91], v20 offset:63488\n"
    "v_mfma_f32_16x16x32_bf16 v[148:151], a[232:235], v[100:103], v[148:151]\n"
    "ds_read_b64_tr_b16 v[92:93], v21 offset:61440\n"
    "s_waitcnt lgkmcnt(9)\n"
    "v_mfma_f32_16x16x32_bf16 v[120:123], a[224:227], v[104:107], v[120:123]\n"
    "ds_read_b64_tr_b16 v[94:95], v21 offset:63488\n"
    "v_mfma_f32_16x16x32_bf16 v[136:139], a[228:231], v[104:107], v[136:139]\n"
    "s_mov_b32 s21, 14581760\n"
    "buffer_load_dwordx4 a[208:211], v51, s[32:35], s21 offen offset:0 sc1\n"
    "v_mfma_f32_16x16x32_bf16 v[152:155], a[232:235], v[104:107], v[152:155]\n"
    "buffer_load_dwordx4 a[212:215], v51, s[32:35], s21 offen offset:1024 sc1\n"
    "s_waitcnt lgkmcnt(8)\n"
    "v_mfma_f32_16x16x32_bf16 v[124:127], a[224:227], v[108:111], v[124:127]\n"
    "v_mfma_f32_16x16x32_bf16 v[140:143], a[228:231], v[108:111], v[140:143]\n"
    "buffer_load_dwordx4 a[216:219], v52, s[32:35], s21 offen offset:0 sc1\n"
    "v_mfma_f32_16x16x32_bf16 v[156:159], a[232:235], v[108:111], v[156:159]\n"
    "s_waitcnt vmcnt(12)\n"
    "v_cmp_ne_u64 vcc, s[72:73], v[14:15]\n"
    "s_nop 1\n"
    "s_cbranch_vccz .Lmk_ok_21\n"
    "s_mov_b32 s78, 0\n"
    ".Lmk_spin_22:\n"
    "buffer_load_dwordx2 v[14:15], v55, s[32:35], s4 offen sc1\n"
    "s_add_u32 s78, s78, 1\n"
    "s_waitcnt vmcnt(0)\n"
    "v_cmp_ne_u64 vcc, s[72:73], v[14:15]\n"
    "s_nop 1\n"
    "s_cbranch_vccz .Lmk_ok_21\n"
    "s_cmp_lt_u32 s78, 20000\n"
    "s_cbranch_scc1 .Lmk_spin_22\n"
    ".Lmk_ok_21:\n"
    "s_waitcnt lgkmcnt(6)\n"
    "v_mfma_f32_16x16x32_bf16 v[6:9], v[58:61], v[80:83], v[6:9]\n"
    "s_add_u32 s5, s19, 0\n"
    "buffer_load_dwordx4 v[168:171], v53, s[32:35], s5 offen sc1\n"
    "v_mfma_f32_16x16x32_bf16 v[112:115], a[240:243], v[80:83], v[112:115]\n"
    "v_mfma_f32_16x16x32_bf16 v[128:131], a[244:247], v[80:83], v[128:131]\n"
    "s_nop 3\n"
    "v_mul_f32 v2, s26, v6\n"
    "v_mfma_f32_16x16x32_bf16 v[144:147], a[248:251], v[80:83], v[144:147]\n"
    "buffer_load_dwordx4 v[172:175], v54, s[32:35], s5 offen sc1\n"
    "v_cvt_pk_bf16_f32 v2, v2, v2\n"
    "s_waitcnt lgkmcnt(4)\n"
    "v_mfma_f32_16x16x32_bf16 v[116:119], a[240:243], v[84:87], v[116:119]\n"
    "v_lshlrev_b32 v2, 16, v2\n"
    "v_mfma_f32_16x16x32_bf16 v[132:135], a[244:247], v[84:87], v[132:135]\n"
    "ds_write_b32 v23, v2 offset:256\n"
    "v_mfma_f32_16x16x32_bf16 v[148:151], a[248:251], v[84:87], v[148:151]\n"
    "s_add_u32 s5, s19, 4096\n"
    "buffer_load_dwordx4 v[176:179], v53, s[32:35], s5 offen sc1\n"
    "v_mov_b32 v17, v6\n"
    "s_waitcnt lgkmcnt(3)\n"
    "v_mfma_f32_16x16x32_bf16 v[120:123], a[240:243], v[88:91], v[120:123]\n"
    "v_cndmask_b32 v16, v252, v253, s[24:25]\n"
    "v_mfma_f32_16x16x32_bf16 v[136:139], a[244:247], v[88:91], v[136:139]\n"
    "v_cndmask_b32 v16, v16, v254, s[30:31]\n"
    "v_mfma_f32_16x16x32_bf16 v[152:155], a[248:251], v[88:91], v[152:155]\n"
    "buffer_load_dwordx4 v[180:183], v54, s[32:35], s5 offen sc1\n"
    "v_cndmask_b32 v16, v16, v255, s[44:45]\n"
    "s_waitcnt lgkmcnt(1)\n"
    "v_mfma_f32_16x16x32_bf16 v[124:127], a[240:243], v[92:95], v[124:127]\n"
    "s_waitcnt lgkmcnt(0)\n"
    "s_barrier\n"
    "v_mfma_f32_16x16x32_bf16 v[140:143], a[244:247], v[92:95], v[140:143]\n"
    "ds_read_b32 v34, v46 offset:256\n"
    "v_mfma_f32_16x16x32_bf16 v[156:159], a[248:251], v[92:95], v[156:159]\n"
    "ds_read_b32 v35, v47 offset:256\n"
    "ds_read_b32 v36, v48 offset:256\n"
    "ds_read_b32 v37, v49 offset:256\n"
    "ds_read_b128 v[72:75], v251 offset:0\n"
    "ds_read_b128 v[76:79], v251 offset:1024\n"
    "s_add_u32 s5, s19, 8192\n"
    "buffer_load_dwordx4 v[184:187], v53, s[32:35], s5 offen sc1\n"
    "buffer_load_dwordx4 v[188:191], v54, s[32:35], s5 offen sc1\n"
    "s_add_u32 s5, s19, 12288\n"
    "buffer_load_dwordx4 v[192:195], v53, s[32:35], s5 offen sc1\n"
    "buffer_load_dwordx4 v[196:199], v54, s[32:35], s5 offen sc1\n"
    "s_waitcnt lgkmcnt(4)\n"
    "v_pk_fma_f32 v[112:113], v[34:35], v[224:225], v[112:113] op_sel:[0,0,0] op_sel_hi:[0,1,1] neg_lo:[1,0,0] neg_hi:[1,0,0]\n"
    "v_pk_fma_f32 v[114:115], v[34:35], v[226:227], v[114:115] op_sel:[0,0,0] op_sel_hi:[0,1,1] neg_lo:[1,0,0] neg_hi:[1,0,0]\n"
    "v_pk_mul_f32 v[112:113], v[112:113], v[114:115]\n"
    "v_cvt_pk_bf16_f32 v38, v112, v113\n"
    "v_pk_fma_f32 v[116:117], v[34:35], v[224:225], v[116:117] op_sel:[1,0,0] op_sel_hi:[1,1,1] neg_lo:[1,0,0] neg_hi:[1,0,0]\n"
    "v_pk_fma_f32 v[118:119], v[34:35], v[226:227], v[118:119] op_sel:[1,0,0] op_sel_hi:[1,1,1] neg_lo:[1,0,0] neg_hi:[1,0,0]\n"
    "v_pk_mul_f32 v[116:117], v[116:117], v[118:119]\n"
    "v_cvt_pk_bf16_f32 v39, v116, v117\n"
    "s_waitcnt lgkmcnt(2)\n"
    "v_pk_fma_f32 v[120:121], v[36:37], v[224:225], v[120:121] op_sel:[0,0,0] op_sel_hi:[0,1,1] neg_lo:[1,0,0] neg_hi:[1,0,0]\n"
    "v_pk_fma_f32 v[122:123], v[36:37], v[226:227], v[122:123] op_sel:[0,0,0] op_sel_hi:[0,1,1] neg_lo:[1,0,0] neg_hi:[1,0,0]\n"
    "v_pk_mul_f32 v[120:121], v[120:121], v[122:123]\n"
    "v_cvt_pk_bf16_f32 v40, v120, v121\n"
    "v_pk_fma_f32 v[124:125], v[36:37], v[224:225], v[124:125] op_sel:[1,0,0] op_sel_hi:[1,1,1] neg_lo:[1,0,0] neg_hi:[1,0,0]\n"
    "v_pk_fma_f32 v[126:127], v[36:37], v[226:227], v[126:127] op_sel:[1,0,0] op_sel_hi:[1,1,1] neg_lo:[1,0,0] neg_hi:[1,0,0]\n"
    "v_pk_mul_f32 v[124:125], v[124:125], v[126:127]\n"
    "v_cvt_pk_bf16_f32 v41, v124, v125\n"
    "v_permlane32_swap_b32 v38, v40\n"
    "s_nop 0\n"
    "v_permlane32_swap_b32 v39, v41\n"
    "s_nop 1\n"
    "v_permlane16_swap_b32 v38, v39\n"
    "v_permlane16_swap_b32 v40, v41\n"
    "s_add_u32 s22, s12, 3670016\n"
    "buffer_store_dwordx4 v[38:41], v13, s[32:35], s22 offen offset:0 sc1\n"
    "v_pk_fma_f32 v[128:129], v[34:35], v[228:229], v[128:129] op_sel:[0,0,0] op_sel_hi:[0,1,1] neg_lo:[1,0,0] neg_hi:[1,0,0]\n"
    "v_pk_fma_f32 v[130:131], v[34:35], v[230:231], v[130:131] op_sel:[0,0,0] op_sel_hi:[0,1,1] neg_lo:[1,0,0] neg_hi:[1,0,0]\n"
    "v_pk_mul_f32 v[128:129], v[128:129], v[130:131]\n"
    "v_cvt_pk_bf16_f32 v64, v128, v129\n"
    "v_pk_fma_f32 v[132:133], v[34:35], v[228:229], v[132:133] op_sel:[1,0,0] op_sel_hi:[1,1,1] neg_lo:[1,0,0] neg_hi:[1,0,0]\n"
    "v_pk_fma_f32 v[134:135], v[34:35], v[230:231], v[134:135] op_sel:[1,0,0] op_sel_hi:[1,1,1] neg_lo:[1,0,0] neg_hi:[1,0,0]\n"
    "v_pk_mul_f32 v[132:133], v[132:133], v[134:135]\n"
    "v_cvt_pk_bf16_f32 v65, v132, v133\n"
    "v_pk_fma_f32 v[136:137], v[36:37], v[228:229], v[136:137] op_sel:[0,0,0] op_sel_hi:[0,1,1] neg_lo:[1,0,0] neg_hi:[1,0,0]\n"
    "v_pk_fma_f32 v[138:139], v[36:37], v[230:231], v[138:139] op_sel:[0,0,0] op_sel_hi:[0,1,1] neg_lo:[1,0,0] neg_hi:[1,0,0]\n"
    "v_pk_mul_f32 v[136:137], v[136:137], v[138:139]\n"
    "v_cvt_pk_bf16_f32 v66, v136, v137\n"
    "v_pk_fma_f32 v[140:141], v[36:37], v[228:229], v[140:141] op_sel:[1,0,0] op_sel_hi:[1,1,1] neg_lo:[1,0,0] neg_hi:[1,0,0]\n"
    "v_pk_fma_f32 v[142:143], v[36:37], v[230:231], v[142:143] op_sel:[1,0,0] op_sel_hi:[1,1,1] neg_lo:[1,0,0] neg_hi:[1,0,0]\n"
    "v_pk_mul_f32 v[140:141], v[140:141], v[142:143]\n"
    "v_cvt_pk_bf16_f32 v67, v140, v141\n"
    "v_permlane32_swap_b32 v64, v66\n"
    "s_nop 0\n"
    "v_permlane32_swap_b32 v65, v67\n"
    "s_nop 1\n"
    "v_permlane16_swap_b32 v64, v65\n"
    "v_permlane16_swap_b32 v66, v67\n"
    "buffer_store_dwordx4 v[64:67], v13, s[32:35], s22 offen offset:1024 sc1\n"
    "v_pk_fma_f32 v[144:145], v[34:35], v[232:233], v[144:145] op_sel:[0,0,0] op_sel_hi:[0,1,1] neg_lo:[1,0,0] neg_hi:[1,0,0]\n"
    "v_pk_fma_f32 v[146:147], v[34:35], v[234:235], v[146:147] op_sel:[0,0,0] op_sel_hi:[0,1,1] neg_lo:[1,0,0] neg_hi:[1,0,0]\n"
    "v_pk_mul_f32 v[144:145], v[144:145], v[146:147]\n"
    "v_cvt_pk_bf16_f32 v38, v144, v145\n"
    "v_pk_fma_f32 v[148:149], v[34:35], v[232:233], v[148:149] op_sel:[1,0,0] op_sel_hi:[1,1,1] neg_lo:[1,0,0] neg_hi:[1,0,0]\n"
    "v_pk_fma_f32 v[150:151], v[34:35], v[234:235], v[150:151] op_sel:[1,0,0] op_sel_hi:[1,1,1] neg_lo:[1,0,0] neg_hi:[1,0,0]\n"
    "v_pk_mul_f32 v[148:149], v[148:149], v[150:151]\n"
    "v_cvt_pk_bf16_f32 v39, v148, v149\n"
    "v_pk_fma_f32 v[152:153], v[36:37], v[232:233], v[152:153] op_sel:[0,0,0] op_sel_hi:[0,1,1] neg_lo:[1,0,0] neg_hi:[1,0,0]\n"
    "v_pk_fma_f32 v[154:155], v[36:37], v[234:235], v[154:155] op_sel:[0,0,0] op_sel_hi:[0,1,1] neg_lo:[1,0,0] neg_hi:[1,0,0]\n"
    "v_pk_mul_f32 v[152:153], v[152:153], v[154:155]\n"
    "v_cvt_pk_bf16_f32 v40, v152, v153\n"
    "v_pk_fma_f32 v[156:157], v[36:37], v[232:233], v[156:157] op_sel:[1,0,0] op_sel_hi:[1,1,1] neg_lo:[1,0,0] neg_hi:[1,0,0]\n"
    "v_pk_fma_f32 v[158:159], v[36:37], v[234:235], v[158:159] op_sel:[1,0,0] op_sel_hi:[1,1,1] neg_lo:[1,0,0] neg_hi:[1,0,0]\n"
    "v_pk_mul_f32 v[156:157], v[156:157], v[158:159]\n"
    "v_cvt_pk_bf16_f32 v41, v156, v157\n"
    "v_permlane32_swap_b32 v38, v40\n"
    "s_nop 0\n"
    "v_permlane32_swap_b32 v39, v41\n"
    "s_nop 1\n"
    "v_permlane16_swap_b32 v38, v39\n"
    "v_permlane16_swap_b32 v40, v41\n"
    "buffer_store_dwordx4 v[38:41], v13, s[32:35], s22 offen offset:2048 sc1\n"
    "s_waitcnt lgkmcnt(1)\n"
    "v_pk_add_f32 v[160:161], v[160:161], v[72:73]\n"
    "v_pk_add_f32 v[162:163], v[162:163], v[74:75]\n"
    "v_pk_fma_f32 v[160:161], v[34:35], v[236:237], v[160:161] op_sel:[0,0,0] op_sel_hi:[0,1,1] neg_lo:[1,0,0] neg_hi:[1,0,0]\n"
    "v_pk_fma_f32 v[162:163], v[34:35], v[238:239], v[162:163] op_sel:[0,0,0] op_sel_hi:[0,1,1] neg_lo:[1,0,0] neg_hi:[1,0,0]\n"
    "v_pk_mul_f32 v[160:161], v[160:161], v[162:163]\n"
    "v_cvt_pk_bf16_f32 v64, v160, v161\n"
    "s_waitcnt lgkmcnt(0)\n"
    "v_pk_add_f32 v[164:165], v[164:165], v[76:77]\n"
    "v_pk_add_f32 v[166:167], v[166:167], v[78:79]\n"
    "v_pk_fma_f32 v[164:165], v[34:35], v[236:237], v[164:165] op_sel:[1,0,0] op_sel_hi:[1,1,1] neg_lo:[1,0,0] neg_hi:[1,0,0]\n"
    "v_pk_fma_f32 v[166:167], v[34:35], v[238:239], v[166:167] op_sel:[1,0,0] op_sel_hi:[1,1,1] neg_lo:[1,0,0] neg_hi:[1,0,0]\n"
    "v_pk_mul_f32 v[164:165], v[164:165], v[166:167]\n"
    "v_cvt_pk_bf16_f32 v65, v164, v165\n"
    "v_permlane32_swap_b32 v64, v66\n"
    "s_nop 0\n"
    "v_permlane32_swap_b32 v65, v67\n"
    "s_nop 1\n"
    "v_permlane16_swap_b32 v64, v65\n"
    "v_permlane16_swap_b32 v66, v67\n"
    "s_add_u32 s22, s16, 3670016\n"
    "s_mov_b32 exec_hi, 0\n"
    "buffer_store_dwordx4 v[64:67], v13, s[32:35], s22 offen offset:0 sc1\n"
    "s_mov_b32 exec_hi, -1\n"
    "s_add_u32 s67, s17, 131072\n"
    "s_mov_b64 exec, s[46:47]\n"
    "buffer_store_dwordx2 v[16:17], v22, s[32:35], s67 offen sc1\n"
    "s_mov_b64 exec, -1\n"
    "s_waitcnt vmcnt(12)\n"
    "v_mfma_f32_16x16x32_bf16 v[112:115], a[0:3], v[168:171], 0\n"
    "s_add_u32 s5, s19, 16384\n"
    "buffer_load_dwordx4 v[200:203], v53, s[32:35], s5 offen sc1\n"
    "v_mfma_f32_16x16x32_bf16 v[120:123], a[4:7], v[168:171], 0\n"
    "v_mfma_f32_16x16x32_bf16 v[128:131], a[8:11], v[168:171], 0\n"
    "buffer_load_dwordx4 v[204:207], v54, s[32:35], s5 offen sc1\n"
    "v_mfma_f32_16x16x32_bf16 v[136:139], a[12:15], v[168:171], 0\n"
    "s_waitcnt vmcnt(13)\n"
    "v_mfma_f32_16x16x32_bf16 v[116:119], a[0:3], v[172:175], 0\n"
    "v_mfma_f32_16x16x32_bf16 v[124:127], a[4:7], v[172:175], 0\n"
    "s_add_u32 s5, s19, 20480\n"
    "buffer_load_dwordx4 v[208:211], v53, s[32:35], s5 offen sc1\n"
    "v_mfma_f32_16x16x32_bf16 v[132:135], a[8:11], v[172:175], 0\n"
    "v_mfma_f32_16x16x32_bf16 v[140:143], a[12:15], v[172:175], 0\n"
    "s_waitcnt vmcnt(13)\n"
    "v_mfma_f32_16x16x32_bf16 v[112:115], a[16:19], v[176:179], v[112:115]\n"
    "buffer_load_dwordx4 v[212:215], v54, s[32:35], s5 offen sc1\n"
    "v_mfma_f32_16x16x32_bf16 v[120:123], a[20:23], v[176:179], v[120:123]\n"
    "v_mfma_f32_16x16x32_bf16 v[128:131], a[24:27], v[176:179], v[128:131]\n"
    "s_add_u32 s67, s23, 0\n"
    "buffer_load_dword v224, v244, s[32:35], s67 offen offset:0 sc1\n"
    "buffer_load_dword v225, v244, s[32:35], s67 offen offset:512 sc1\n"
    "buffer_load_dword v226, v244, s[32:35], s67 offen offset:1024 sc1\n"
    "buffer_load_dwordx2 v[228:229], v244, s[32:35], s67 offen offset:1536 sc1\n"
    "v_mfma_f32_16x16x32_bf16 v[136:139], a[28:31], v[176:179], v[136:139]\n"
    "s_waitcnt vmcnt(17)\n"
    "v_mfma_f32_16x16x32_bf16 v[116:119], a[16:19], v[180:183], v[116:119]\n"
    "v_mfma_f32_16x16x32_bf16 v[124:127], a[20:23], v[180:183], v[124:127]\n"
    "s_add_u32 s5, s19, 24576\n"
    "buffer_load_dwordx4 v[216:219], v53, s[32:35], s5 offen sc1\n"
    "v_mfma_f32_16x16x32_bf16 v[132:135], a[24:27], v[180:183], v[132:135]\n"
    "v_mfma_f32_16x16x32_bf16 v[140:143], a[28:31], v[180:183], v[140:143]\n"
    "s_waitcnt vmcnt(17)\n"
    "v_mfma_f32_16x16x32_bf16 v[112:115], a[32:35], v[184:187], v[112:115]\n"
    "buffer_load_dwordx4 v[220:223], v54, s[32:35], s5 offen sc1\n"
    "v_mfma_f32_16x16x32_bf16 v[120:123], a[36:39], v[184:187], v[120:123]\n"
    "v_mfma_f32_16x16x32_bf16 v[128:131], a[40:43], v[184:187], v[128:131]\n"
    "s_add_u32 s5, s19, 28672\n"
    "buffer_load_dwordx4 a[224:227], v53, s[32:35], s5 offen sc1\n"
    "v_mfma_f32_16x16x32_bf16 v[136:139], a[44:47], v[184:187], v[136:139]\n"
    "s_waitcnt vmcnt(18)\n"
    "v_mfma_f32_16x16x32_bf16 v[116:119], a[32:35], v[188:191], v[116:119]\n"
    "v_mfma_f32_16x16x32_bf16 v[124:127], a[36:39], v[188:191], v[124:127]\n"
    "buffer_load_dwordx4 a[228:231], v54, s[32:35], s5 offen sc1\n"
    "v_mfma_f32_16x16x32_bf16 v[132:135], a[40:43], v[188:191], v[132:135]\n"
    "v_mfma_f32_16x16x32_bf16 v[140:143], a[44:47], v[188:191], v[140:143]\n"
    "s_waitcnt vmcnt(12)\n"
    "s_barrier\n"
    "s_add_u32 s67, s18, 1572864\n"
    "s_mov_b64 exec, 1\n"
    "buffer_store_dwordx2 v[62:63], v50, s[32:35], s67 offen sc1\n"
    "s_mov_b64 exec, -1\n"
    "v_mfma_f32_16x16x32_bf16 v[112:115], a[48:51], v[192:195], v[112:115]\n"
    "s_add_u32 s5, s19, 32768\n"
    "buffer_load_dwordx4 a[232:235], v53, s[32:35], s5 offen sc1\n"
    "v_mfma_f32_16x16x32_bf16 v[120:123], a[52:55], v[192:195], v[120:123]\n"
    "v_mfma_f32_16x16x32_bf16 v[128:131], a[56:59], v[192:195], v[128:131]\n"
    "buffer_load_dwordx4 a[236:239], v54, s[32:35], s5 offen sc1\n"
    "v_mfma_f32_16x16x32_bf16 v[136:139], a[60:63], v[192:195], v[136:139]\n"
    "v_mfma_f32_16x16x32_bf16 v[116:119], a[48:51], v[196:199], v[116:119]\n"
    "v_mfma_f32_16x16x32_bf16 v[124:127], a[52:55], v[196:199], v[124:127]\n"
    "s_add_u32 s5, s19, 36864\n"
    "buffer_load_dwordx4 a[240:243], v53, s[32:35], s5 offen sc1\n"
    "v_mfma_f32_16x16x32_bf16 v[132:135], a[56:59], v[196:199], v[132:135]\n"
    "v_mfma_f32_16x16x32_bf16 v[140:143], a[60:63], v[196:199], v[140:143]\n"
    "s_waitcnt vmcnt(15)\n"
    "v_mfma_f32_16x16x32_bf16 v[112:115], a[64:67], v[200:203], v[112:115]\n"
    "buffer_load_dwordx4 a[244:247], v54, s[32:35], s5 offen sc1\n"
    "v_mfma_f32_16x16x32_bf16 v[120:123], a[68:71], v[200:203], v[120:123]\n"
    "v_mfma_f32_16x16x32_bf16 v[128:131], a[72:75], v[200:203], v[128:131]\n"
    "s_add_u32 s5, s19, 40960\n"
    "buffer_load_dwordx4 a[248:251], v53, s[32:35], s5 offen sc1\n"
    "v_mfma_f32_16x16x32_bf16 v[136:139], a[76:79], v[200:203], v[136:139]\n"
    "s_waitcnt vmcnt(16)\n"
    "v_mfma_f32_16x16x32_bf16 v[116:119], a[64:67], v[204:207], v[116:119]\n"
    "v_mfma_f32_16x16x32_bf16 v[124:127], a[68:71], v[204:207], v[124:127]\n"
    "buffer_load_dwordx4 a[252:255], v54, s[32:35], s5 offen sc1\n"
    "v_mfma_f32_16x16x32_bf16 v[132:135], a[72:75], v[204:207], v[132:135]\n"
    "v_mfma_f32_16x16x32_bf16 v[140:143], a[76:79], v[204:207], v[140:143]\n"
    "s_waitcnt vmcnt(16)\n"
    "v_mfma_f32_16x16x32_bf16 v[112:115], a[80:83], v[208:211], v[112:115]\n"
    "s_add_u32 s5, s19, 45056\n"
    "buffer_load_dwordx4 v[96:99], v53, s[32:35], s5 offen sc1\n"
    "v_mfma_f32_16x16x32_bf16 v[120:123], a[84:87], v[208:211], v[120:123]\n"
    "v_mfma_f32_16x16x32_bf16 v[128:131], a[88:91], v[208:211], v[128:131]\n"
    "buffer_load_dwordx4 v[100:103], v54, s[32:35], s5 offen sc1\n"
    "v_mfma_f32_16x16x32_bf16 v[136:139], a[92:95], v[208:211], v[136:139]\n"
    "s_waitcnt vmcnt(17)\n"
    "v_mfma_f32_16x16x32_bf16 v[116:119], a[80:83], v[212:215], v[116:119]\n"
    "v_mfma_f32_16x16x32_bf16 v[124:127], a[84:87], v[212:215], v[124:127]\n"
    "s_add_u32 s5, s19, 49152\n"
    "buffer_load_dwordx4 v[104:107], v53, s[32:35], s5 offen sc1\n"
    "v_mfma_f32_16x16x32_bf16 v[132:135], a[88:91], v[212:215], v[132:135]\n"
    "v_mfma_f32_16x16x32_bf16 v[140:143], a[92:95], v[212:215], v[140:143]\n"
    "s_waitcnt vmcnt(13)\n"
    "v_mfma_f32_16x16x32_bf16 v[112:115], a[96:99], v[216:219], v[112:115]\n"
    "buffer_load_dwordx4 v[108:111], v54, s[32:35], s5 offen sc1\n"
    "v_mfma_f32_16x16x32_bf16 v[120:123], a[100:103], v[216:219], v[120:123]\n"
    "v_mfma_f32_16x16x32_bf16 v[128:131], a[104:107], v[216:219], v[128:131]\n"
    "s_add_u32 s5, s19, 53248\n"
    "buffer_load_dwordx4 v[80:83], v53, s[32:35], s5 offen sc1\n"
    "v_mfma_f32_16x16x32_bf16 v[136:139], a[108:111], v[216:219], v[136:139]\n"
    "s_waitcnt vmcnt(14)\n"
    "v_mfma_f32_16x16x32_bf16 v[116:119], a[96:99], v[220:223], v[116:119]\n"
    "v_mfma_f32_16x16x32_bf16 v[124:127], a[100:103], v[220:223], v[124:127]\n"
    "buffer_load_dwordx4 v[84:87], v54, s[32:35], s5 offen sc1\n"
    "v_mfma_f32_16x16x32_bf16 v[132:135], a[104:107], v[220:223], v[132:135]\n"
    "v_mfma_f32_16x16x32_bf16 v[140:143], a[108:111], v[220:223], v[140:143]\n"
    "s_waitcnt vmcnt(14)\n"
    "v_mfma_f32_16x16x32_bf16 v[112:115], a[112:115], a[224:227], v[112:115]\n"
    "v_mfma_f32_16x16x32_bf16 v[120:123], a[116:119], a[224:227], v[120:123]\n"
    "v_mfma_f32_16x16x32_bf16 v[128:131], a[120:123], a[224:227], v[128:131]\n"
    "v_mfma_f32_16x16x32_bf16 v[136:139], a[124:127], a[224:227], v[136:139]\n"
    "s_waitcnt vmcnt(13)\n"
    "v_mfma_f32_16x16x32_bf16 v[116:119], a[112:115], a[228:231], v[116:119]\n"
    "v_mfma_f32_16x16x32_bf16 v[124:127], a[116:119], a[228:231], v[124:127]\n"
    "v_mfma_f32_16x16x32_bf16 v[132:135], a[120:123], a[228:231], v[132:135]\n"
    "v_mfma_f32_16x16x32_bf16 v[140:143], a[124:127], a[228:231], v[140:143]\n"
    "s_waitcnt vmcnt(11)\n"
    "v_mfma_f32_16x16x32_bf16 v[112:115], a[128:131], a[232:235], v[112:115]\n"
    "v_mfma_f32_16x16x32_bf16 v[120:123], a[132:135], a[232:235], v[120:123]\n"
    "v_mfma_f32_16x16x32_bf16 v[128:131], a[136:139], a[232:235], v[128:131]\n"
    "v_mfma_f32_16x16x32_bf16 v[136:139], a[140:143], a[232:235], v[136:139]\n"
    "s_waitcnt vmcnt(10)\n"
    "v_mfma_f32_16x16x32_bf16 v[116:119], a[128:131], a[236:239], v[116:119]\n"
    "v_mfma_f32_16x16x32_bf16 v[124:127], a[132:135], a[236:239], v[124:127]\n"
    "v_mfma_f32_16x16x32_bf16 v[132:135], a[136:139], a[236:239], v[132:135]\n"
    "v_mfma_f32_16x16x32_bf16 v[140:143], a[140:143], a[236:239], v[140:143]\n"
    "s_waitcnt vmcnt(9)\n"
    "v_mfma_f32_16x16x32_bf16 v[112:115], a[144:147], a[240:243], v[112:115]\n"
    "v_mfma_f32_16x16x32_bf16 v[120:123], a[148:151], a[240:243], v[120:123]\n"
    "v_mfma_f32_16x16x32_bf16 v[128:131], a[152:155], a[240:243], v[128:131]\n"
    "v_mfma_f32_16x16x32_bf16 v[136:139], a[156:159], a[240:243], v[136:139]\n"
    "s_waitcnt vmcnt(8)\n"
    "v_mfma_f32_16x16x32_bf16 v[116:119], a[144:147], a[244:247], v[116:119]\n"
    "v_mfma_f32_16x16x32_bf16 v[124:127], a[148:151], a[244:247], v[124:127]\n"
    "v_mfma_f32_16x16x32_bf16 v[132:135], a[152:155], a[244:247], v[132:135]\n"
    "v_mfma_f32_16x16x32_bf16 v[140:143], a[156:159], a[244:247], v[140:143]\n"
    "s_add_u32 s4, s20, 1572864\n"
    "buffer_load_dwordx2 v[14:15], v55, s[32:35], s4 offen sc1\n"
    "s_waitcnt vmcnt(8)\n"
    "v_mfma_f32_16x16x32_bf16 v[112:115], a[160:163], a[248:251], v[112:115]\n"
    "ds_read_b64_tr_b16 v[58:59], v57 offset:0\n"
    "ds_read_b64_tr_b16 v[60:61], v57 offset:1024\n"
    "v_mfma_f32_16x16x32_bf16 v[120:123], a[164:167], a[248:251], v[120:123]\n"
    "v_add_f32 v224, v224, v225\n"
    "v_add_f32 v226, v226, v228\n"
    "v_mfma_f32_16x16x32_bf16 v[128:131], a[168:171], a[248:251], v[128:131]\n"
    "v_add_f32 v224, v224, v226\n"
    "v_mov_b32 v2, v224\n"
    "v_mfma_f32_16x16x32_bf16 v[136:139], a[172:175], a[248:251], v[136:139]\n"
    "v_mul_f32 v229, s26, v229\n"
    "s_waitcnt lgkmcnt(1)\n"
    "v_lshlrev_b32 v236, 16, v58\n"
    "s_waitcnt vmcnt(7)\n"
    "v_mfma_f32_16x16x32_bf16 v[116:119], a[160:163], a[252:255], v[116:119]\n"
    "v_permlane16_swap_b32 v224, v2\n"
    "v_add_f32 v224, v224, v2\n"
    "v_mfma_f32_16x16x32_bf16 v[124:127], a[164:167], a[252:255], v[124:127]\n"
    "v_mov_b32 v2, v224\n"
    "v_and_b32 v237, 0xffff0000, v58\n"
    "v_mfma_f32_16x16x32_bf16 v[132:135], a[168:171], a[252:255], v[132:135]\n"
    "v_mfma_f32_16x16x32_bf16 v[140:143], a[172:175], a[252:255], v[140:143]\n"
    "s_waitcnt vmcnt(6)\n"
    "v_mfma_f32_16x16x32_bf16 v[112:115], a[176:179], v[96:99], v[112:115]\n"
    "v_lshlrev_b32 v238, 16, v59\n"
    "v_permlane32_swap_b32 v224, v2\n"
    "v_mfma_f32_16x16x32_bf16 v[120:123], a[180:183], v[96:99], v[120:123]\n"
    "v_add_f32 v224, v224, v2\n"
    "v_mul_f32 v224, s26, v224\n"
    "v_mfma_f32_16x16x32_bf16 v[128:131], a[184:187], v[96:99], v[128:131]\n"
    "v_fma_f32 v224, -v229, v229, v224\n"
    "v_max_f32 v224, 0, v224\n"
    "v_mfma_f32_16x16x32_bf16 v[136:139], a[188:191], v[96:99], v[136:139]\n"
    "v_cvt_pk_bf16_f32 v224, v224, v224\n"
    "v_lshlrev_b32 v224, 16, v224\n"
    "s_waitcnt vmcnt(5)\n"
    "v_mfma_f32_16x16x32_bf16 v[116:119], a[176:179], v[100:103], v[116:119]\n"
    "v_add_f32 v224, s28, v224\n"
    "v_cvt_pk_bf16_f32 v224, v224, v224\n"
    "v_mfma_f32_16x16x32_bf16 v[124:127], a[180:183], v[100:103], v[124:127]\n"
    "v_lshlrev_b32 v224, 16, v224\n"
    "v_sqrt_f32 v224, v224\n"
    "v_mfma_f32_16x16x32_bf16 v[132:135], a[184:187], v[100:103], v[132:135]\n"
    "v_mfma_f32_16x16x32_bf16 v[140:143], a[188:191], v[100:103], v[140:143]\n"
    "s_waitcnt vmcnt(4)\n"
    "v_mfma_f32_16x16x32_bf16 v[112:115], a[192:195], v[104:107], v[112:115]\n"
    "v_and_b32 v239, 0xffff0000, v59\n"
    "v_cvt_pk_bf16_f32 v224, v224, v224\n"
    "v_mfma_f32_16x16x32_bf16 v[120:123], a[196:199], v[104:107], v[120:123]\n"
    "v_lshlrev_b32 v224, 16, v224\n"
    "v_rcp_f32 v6, v224\n"
    "v_mfma_f32_16x16x32_bf16 v[128:131], a[200:203], v[104:107], v[128:131]\n"
    "s_waitcnt lgkmcnt(0)\n"
    "v_lshlrev_b32 v240, 16, v60\n"
    "v_mul_f32 v6, v6, v6\n"
    "v_mfma_f32_16x16x32_bf16 v[136:139], a[204:207], v[104:107], v[136:139]\n"
    "v_and_b32 v241, 0xffff0000, v60\n"
    "v_lshlrev_b32 v242, 16, v61\n"
    "s_waitcnt vmcnt(3)\n"
    "v_mfma_f32_16x16x32_bf16 v[116:119], a[192:195], v[108:111], v[116:119]\n"
    "v_and_b32 v243, 0xffff0000, v61\n"
    "v_mfma_f32_16x16x32_bf16 v[124:127], a[196:199], v[108:111], v[124:127]\n"
    "v_mfma_f32_16x16x32_bf16 v[132:135], a[200:203], v[108:111], v[132:135]\n"
    "v_mfma_f32_16x16x32_bf16 v[140:143], a[204:207], v[108:111], v[140:143]\n"
    "s_waitcnt vmcnt(0)\n"
    "v_cmp_ne_u64 vcc, s[72:73], v[14:15]\n"
    "s_nop 1\n"
    "s_cbranch_vccz .Lmk_ok_23\n"
    "s_mov_b32 s78, 0\n"
    ".Lmk_spin_24:\n"
    "buffer_load_dwordx2 v[14:15], v55, s[32:35], s4 offen sc1\n"
    "s_add_u32 s78, s78, 1\n"
    "s_waitcnt vmcnt(0)\n"
    "v_cmp_ne_u64 vcc, s[72:73], v[14:15]\n"
    "s_nop 1\n"
    "s_cbranch_vccz .Lmk_ok_23\n"
    "s_cmp_lt_u32 s78, 20000\n"
    "s_cbranch_scc1 .Lmk_spin_24\n"
    ".Lmk_ok_23:\n"
    "v_mfma_f32_16x16x32_bf16 v[112:115], a[208:211], v[80:83], v[112:115]\n"
    "s_add_u32 s5, s19, 3670016\n"
    "buffer_load_dwordx4 v[168:171], v53, s[32:35], s5 offen sc1\n"
    "v_mfma_f32_16x16x32_bf16 v[120:123], a[212:215], v[80:83], v[120:123]\n"
    "buffer_load_dwordx4 v[172:175], v54, s[32:35], s5 offen sc1\n"
    "v_mfma_f32_16x16x32_bf16 v[128:131], a[216:219], v[80:83], v[128:131]\n"
    "s_add_u32 s5, s19, 3674112\n"
    "buffer_load_dwordx4 v[176:179], v53, s[32:35], s5 offen sc1\n"
    "v_mfma_f32_16x16x32_bf16 v[136:139], a[220:223], v[80:83], v[136:139]\n"
    "v_mfma_f32_16x16x32_bf16 v[116:119], a[208:211], v[84:87], v[116:119]\n"
    "buffer_load_dwordx4 v[180:183], v54, s[32:35], s5 offen sc1\n"
    "v_mfma_f32_16x16x32_bf16 v[124:127], a[212:215], v[84:87], v[124:127]\n"
    "s_add_u32 s5, s19, 3678208\n"
    "buffer_load_dwordx4 v[184:187], v53, s[32:35], s5 offen sc1\n"
    "v_mfma_f32_16x16x32_bf16 v[132:135], a[216:219], v[84:87], v[132:135]\n"
    "buffer_load_dwordx4 v[188:191], v54, s[32:35], s5 offen sc1\n"
    "v_mfma_f32_16x16x32_bf16 v[140:143], a[220:223], v[84:87], v[140:143]\n"
    "s_nop 0\n"
    "ds_write_b128 v246, v[116:119] offset:0\n"
    "s_nop 0\n"
    "ds_write_b128 v246, v[124:127] offset:1024\n"
    "ds_write_b128 v247, v[128:131] offset:0\n"
    "s_nop 0\n"
    "ds_write_b128 v248, v[132:135] offset:0\n"
    "ds_write_b128 v247, v[136:139] offset:1024\n"
    "ds_write_b128 v248, v[140:143] offset:1024\n"
    "s_add_u32 s5, s19, 3682304\n"
    "buffer_load_dwordx4 v[192:195], v53, s[32:35], s5 offen sc1\n"
    "buffer_load_dwordx4 v[196:199], v54, s[32:35], s5 offen sc1\n"
    "s_add_u32 s5, s19, 3686400\n"
    "buffer_load_dwordx4 v[200:203], v53, s[32:35], s5 offen sc1\n"
    "buffer_load_dwordx4 v[204:207], v54, s[32:35], s5 offen sc1\n"
    "s_waitcnt vmcnt(9)\n"
    "v_mfma_f32_16x16x32_bf16 v[252:255], a[0:3], v[168:171], 0\n"
    "s_add_u32 s5, s19, 3690496\n"
    "buffer_load_dwordx4 v[208:211], v53, s[32:35], s5 offen sc1\n"
    "v_mfma_f32_16x16x32_bf16 v[16:19], a[4:7], v[168:171], 0\n"
    "v_mfma_f32_16x16x32_bf16 v[64:67], a[8:11], v[168:171], 0\n"
    "v_mfma_f32_16x16x32_bf16 v[72:75], a[12:15], v[168:171], 0\n"
    "s_waitcnt vmcnt(9)\n"
    "v_mfma_f32_16x16x32_bf16 v[8:11], a[0:3], v[172:175], 0\n"
    "buffer_load_dwordx4 v[212:215], v54, s[32:35], s5 offen sc1\n"
    "v_mfma_f32_16x16x32_bf16 v[20:23], a[4:7], v[172:175], 0\n"
    "v_mfma_f32_16x16x32_bf16 v[68:71], a[8:11], v[172:175], 0\n"
    "v_mfma_f32_16x16x32_bf16 v[76:79], a[12:15], v[172:175], 0\n"
    "s_waitcnt vmcnt(9)\n"
    "v_mfma_f32_16x16x32_bf16 v[252:255], a[16:19], v[176:179], v[252:255]\n"
    "s_add_u32 s5, s19, 3694592\n"
    "buffer_load_dwordx4 v[216:219], v53, s[32:35], s5 offen sc1\n"
    "v_mfma_f32_16x16x32_bf16 v[16:19], a[20:23], v[176:179], v[16:19]\n"
    "v_mfma_f32_16x16x32_bf16 v[64:67], a[24:27], v[176:179], v[64:67]\n"
    "v_mfma_f32_16x16x32_bf16 v[72:75], a[28:31], v[176:179], v[72:75]\n"
    "s_waitcnt vmcnt(9)\n"
    "v_mfma_f32_16x16x32_bf16 v[8:11], a[16:19], v[180:183], v[8:11]\n"
    "buffer_load_dwordx4 v[220:223], v54, s[32:35], s5 offen sc1\n"
    "v_mfma_f32_16x16x32_bf16 v[20:23], a[20:23], v[180:183], v[20:23]\n"
    "v_mfma_f32_16x16x32_bf16 v[68:71], a[24:27], v[180:183], v[68:71]\n"
    "v_mfma_f32_16x16x32_bf16 v[76:79], a[28:31], v[180:183], v[76:79]\n"
    "s_waitcnt vmcnt(9)\n"
    "v_mfma_f32_16x16x32_bf16 v[252:255], a[32:35], v[184:187], v[252:255]\n"
    "s_add_u32 s67, s23, 131072\n"
    "buffer_load_dword v230, v244, s[32:35], s67 offen offset:0 sc1\n"
    "buffer_load_dword v231, v244, s[32:35], s67 offen offset:512 sc1\n"
    "buffer_load_dword v232, v244, s[32:35], s67 offen offset:1024 sc1\n"
    "buffer_load_dwordx2 v[234:235], v244, s[32:35], s67 offen offset:1536 sc1\n"
    "v_mfma_f32_16x16x32_bf16 v[16:19], a[36:39], v[184:187], v[16:19]\n"
    "v_mfma_f32_16x16x32_bf16 v[64:67], a[40:43], v[184:187], v[64:67]\n"
    "v_mfma_f32_16x16x32_bf16 v[72:75], a[44:47], v[184:187], v[72:75]\n"
    "s_waitcnt vmcnt(12)\n"
    "v_mfma_f32_16x16x32_bf16 v[8:11], a[32:35], v[188:191], v[8:11]\n"
    "s_add_u32 s5, s19, 3698688\n"
    "buffer_load_dwordx4 a[224:227], v53, s[32:35], s5 offen sc1\n"
    "v_mfma_f32_16x16x32_bf16 v[20:23], a[36:39], v[188:191], v[20:23]\n"
    "v_mfma_f32_16x16x32_bf16 v[68:71], a[40:43], v[188:191], v[68:71]\n"
    "v_mfma_f32_16x16x32_bf16 v[76:79], a[44:47], v[188:191], v[76:79]\n"
    "s_waitcnt vmcnt(12)\n"
    "v_mfma_f32_16x16x32_bf16 v[252:255], a[48:51], v[192:195], v[252:255]\n"
    "buffer_load_dwordx4 a[228:231], v54, s[32:35], s5 offen sc1\n"
    "v_mfma_f32_16x16x32_bf16 v[16:19], a[52:55], v[192:195], v[16:19]\n"
    "v_mfma_f32_16x16x32_bf16 v[64:67], a[56:59], v[192:195], v[64:67]\n"
    "v_mfma_f32_16x16x32_bf16 v[72:75], a[60:63], v[192:195], v[72:75]\n"
    "s_waitcnt vmcnt(12)\n"
    "v_mfma_f32_16x16x32_bf16 v[8:11], a[48:51], v[196:199], v[8:11]\n"
    "s_add_u32 s5, s19, 3702784\n"
    "buffer_load_dwordx4 a[232:235], v53, s[32:35], s5 offen sc1\n"
    "v_mfma_f32_16x16x32_bf16 v[20:23], a[52:55], v[196:199], v[20:23]\n"
    "v_mfma_f32_16x16x32_bf16 v[68:71], a[56:59], v[196:199], v[68:71]\n"
    "v_mfma_f32_16x16x32_bf16 v[76:79], a[60:63], v[196:199], v[76:79]\n"
    "s_waitcnt lgkmcnt(0)\n"
    "s_barrier\n"
    "s_waitcnt vmcnt(12)\n"
    "v_mfma_f32_16x16x32_bf16 v[252:255], a[64:67], v[200:203], v[252:255]\n"
    "buffer_load_dwordx4 a[236:239], v54, s[32:35], s5 offen sc1\n"
    "ds_read_b128 v[144:147], v249 offset:0\n"
    "v_mfma_f32_16x16x32_bf16 v[16:19], a[68:71], v[200:203], v[16:19]\n"
    "ds_read_b128 v[148:151], v249 offset:1024\n"
    "v_mfma_f32_16x16x32_bf16 v[64:67], a[72:75], v[200:203], v[64:67]\n"
    "ds_read_b128 v[152:155], v249 offset:2048\n"
    "v_mfma_f32_16x16x32_bf16 v[72:75], a[76:79], v[200:203], v[72:75]\n"
    "ds_read_b128 v[156:159], v249 offset:3072\n"
    "s_waitcnt vmcnt(12)\n"
    "v_mfma_f32_16x16x32_bf16 v[8:11], a[64:67], v[204:207], v[8:11]\n"
    "s_add_u32 s5, s19, 3706880\n"
    "buffer_load_dwordx4 a[240:243], v53, s[32:35], s5 offen sc1\n"
    "ds_read_b128 v[160:163], v249 offset:4096\n"
    "v_mfma_f32_16x16x32_bf16 v[20:23], a[68:71], v[204:207], v[20:23]\n"
    "ds_read_b128 v[164:167], v249 offset:5120\n"
    "v_mfma_f32_16x16x32_bf16 v[68:71], a[72:75], v[204:207], v[68:71]\n"
    "v_mfma_f32_16x16x32_bf16 v[76:79], a[76:79], v[204:207], v[76:79]\n"
    "s_waitcnt vmcnt(12)\n"
    "v_mfma_f32_16x16x32_bf16 v[252:255], a[80:83], v[208:211], v[252:255]\n"
    "buffer_load_dwordx4 a[244:247], v54, s[32:35], s5 offen sc1\n"
    "s_waitcnt lgkmcnt(5)\n"
    "v_add_f32 v112, v112, v144\n"
    "v_add_f32 v113, v113, v145\n"
    "v_mfma_f32_16x16x32_bf16 v[16:19], a[84:87], v[208:211], v[16:19]\n"
    "v_add_f32 v114, v114, v146\n"
    "v_add_f32 v115, v115, v147\n"
    "v_mfma_f32_16x16x32_bf16 v[64:67], a[88:91], v[208:211], v[64:67]\n"
    "s_waitcnt lgkmcnt(4)\n"
    "v_add_f32 v120, v120, v148\n"
    "v_add_f32 v121, v121, v149\n"
    "v_mfma_f32_16x16x32_bf16 v[72:75], a[92:95], v[208:211], v[72:75]\n"
    "v_add_f32 v122, v122, v150\n"
    "v_add_f32 v123, v123, v151\n"
    "s_waitcnt vmcnt(12)\n"
    "v_mfma_f32_16x16x32_bf16 v[8:11], a[80:83], v[212:215], v[8:11]\n"
    "s_add_u32 s5, s19, 3710976\n"
    "buffer_load_dwordx4 a[248:251], v53, s[32:35], s5 offen sc1\n"
    "s_waitcnt lgkmcnt(3)\n"
    "v_add_f32 v112, v112, v152\n"
    "v_add_f32 v113, v113, v153\n"
    "v_mfma_f32_16x16x32_bf16 v[20:23], a[84:87], v[212:215], v[20:23]\n"
    "v_add_f32 v114, v114, v154\n"
    "v_add_f32 v115, v115, v155\n"
    "v_mfma_f32_16x16x32_bf16 v[68:71], a[88:91], v[212:215], v[68:71]\n"
    "s_waitcnt lgkmcnt(2)\n"
    "v_add_f32 v120, v120, v156\n"
    "v_add_f32 v121, v121, v157\n"
    "v_mfma_f32_16x16x32_bf16 v[76:79], a[92:95], v[212:215], v[76:79]\n"
    "s_waitcnt vmcnt(12)\n"
    "v_mfma_f32_16x16x32_bf16 v[64:67], a[104:107], v[216:219], v[64:67]\n"
    "buffer_load_dwordx4 a[252:255], v54, s[32:35], s5 offen sc1\n"
    "v_add_f32 v122, v122, v158\n"
    "v_add_f32 v123, v123, v159\n"
    "v_mfma_f32_16x16x32_bf16 v[72:75], a[108:111], v[216:219], v[72:75]\n"
    "s_waitcnt lgkmcnt(1)\n"
    "v_add_f32 v112, v112, v160\n"
    "v_add_f32 v113, v113, v161\n"
    "s_waitcnt vmcnt(12)\n"
    "v_mfma_f32_16x16x32_bf16 v[8:11], a[96:99], v[220:223], v[8:11]\n"
    "v_add_f32 v114, v114, v162\n"
    "v_add_f32 v115, v115, v163\n"
    "v_mfma_f32_16x16x32_bf16 v[20:23], a[100:103], v[220:223], v[20:23]\n"
    "s_add_u32 s5, s19, 3715072\n"
    "buffer_load_dwordx4 v[96:99], v53, s[32:35], s5 offen sc1\n"
    "s_waitcnt lgkmcnt(0)\n"
    "v_add_f32 v120, v120, v164\n"
    "v_add_f32 v121, v121, v165\n"
    "v_mfma_f32_16x16x32_bf16 v[68:71], a[104:107], v[220:223], v[68:71]\n"
    "v_add_f32 v122, v122, v166\n"
    "v_add_f32 v123, v123, v167\n"
    "v_mfma_f32_16x16x32_bf16 v[76:79], a[108:111], v[220:223], v[76:79]\n"
    "v_fma_f32 v112, v112, v6, v236\n"
    "v_fma_f32 v113, v113, v6, v237\n"
    "v_cvt_pk_bf16_f32 v26, v112, v113\n"
    "v_fma_f32 v114, v114, v6, v238\n"
    "s_waitcnt vmcnt(8)\n"
    "v_mfma_f32_16x16x32_bf16 v[64:67], a[120:123], a[224:227], v[64:67]\n"
    "buffer_load_dwordx4 v[100:103], v54, s[32:35], s5 offen sc1\n"
    "v_fma_f32 v115, v115, v6, v239\n"
    "v_cvt_pk_bf16_f32 v27, v114, v115\n"
    "v_mfma_f32_16x16x32_bf16 v[72:75], a[124:127], a[224:227], v[72:75]\n"
    "v_fma_f32 v120, v120, v6, v240\n"
    "v_fma_f32 v121, v121, v6, v241\n"
    "s_waitcnt vmcnt(8)\n"
    "v_mfma_f32_16x16x32_bf16 v[8:11], a[112:115], a[228:231], v[8:11]\n"
    "v_cvt_pk_bf16_f32 v28, v120, v121\n"
    "v_fma_f32 v122, v122, v6, v242\n"
    "v_mfma_f32_16x16x32_bf16 v[20:23], a[116:119], a[228:231], v[20:23]\n"
    "s_add_u32 s5, s19, 3719168\n"
    "buffer_load_dwordx4 v[104:107], v53, s[32:35], s5 offen sc1\n"
    "v_fma_f32 v123, v123, v6, v243\n"
    "v_cvt_pk_bf16_f32 v29, v122, v123\n"
    "v_mfma_f32_16x16x32_bf16 v[68:71], a[120:123], a[228:231], v[68:71]\n"
    "v_permlane16_swap_b32 v26, v28\n"
    "v_permlane16_swap_b32 v27, v29\n"
    "v_mfma_f32_16x16x32_bf16 v[76:79], a[124:127], a[228:231], v[76:79]\n"
    "s_mov_b32 s67, 0\n"
    "buffer_store_dwordx4 v[26:29], v245, s[52:55], s67 offen\n"
    "s_waitcnt vmcnt(9)\n"
    "v_mfma_f32_16x16x32_bf16 v[64:67], a[136:139], a[232:235], v[64:67]\n"
    "buffer_load_dwordx4 v[108:111], v54, s[32:35], s5 offen sc1\n"
    "v_mfma_f32_16x16x32_bf16 v[72:75], a[140:143], a[232:235], v[72:75]\n"
    "s_waitcnt vmcnt(9)\n"
    "v_mfma_f32_16x16x32_bf16 v[8:11], a[128:131], a[236:239], v[8:11]\n"
    "v_mfma_f32_16x16x32_bf16 v[20:23], a[132:135], a[236:239], v[20:23]\n"
    "s_add_u32 s5, s19, 3723264\n"
    "buffer_load_dwordx4 v[80:83], v53, s[32:35], s5 offen sc1\n"
    "v_mfma_f32_16x16x32_bf16 v[68:71], a[136:139], a[236:239], v[68:71]\n"
    "v_mfma_f32_16x16x32_bf16 v[76:79], a[140:143], a[236:239], v[76:79]\n"
    "s_waitcnt vmcnt(9)\n"
    "v_mfma_f32_16x16x32_bf16 v[64:67], a[152:155], a[240:243], v[64:67]\n"
    "buffer_load_dwordx4 v[84:87], v54, s[32:35], s5 offen sc1\n"
    "v_mfma_f32_16x16x32_bf16 v[72:75], a[156:159], a[240:243], v[72:75]\n"
    "s_waitcnt vmcnt(9)\n"
    "v_mfma_f32_16x16x32_bf16 v[8:11], a[144:147], a[244:247], v[8:11]\n"
    "v_mfma_f32_16x16x32_bf16 v[20:23], a[148:151], a[244:247], v[20:23]\n"
    "v_mfma_f32_16x16x32_bf16 v[68:71], a[152:155], a[244:247], v[68:71]\n"
    "v_mfma_f32_16x16x32_bf16 v[76:79], a[156:159], a[244:247], v[76:79]\n"
    "s_waitcnt vmcnt(8)\n"
    "v_mfma_f32_16x16x32_bf16 v[64:67], a[168:171], a[248:251], v[64:67]\n"
    "ds_read_b64_tr_b16 v[58:59], v57 offset:4096\n"
    "ds_read_b64_tr_b16 v[60:61], v57 offset:5120\n"
    "v_mfma_f32_16x16x32_bf16 v[72:75], a[172:175], a[248:251], v[72:75]\n"
    "v_add_f32 v230, v230, v231\n"
    "v_add_f32 v232, v232, v234\n"
    "s_waitcnt vmcnt(7)\n"
    "v_mfma_f32_16x16x32_bf16 v[8:11], a[160:163], a[252:255], v[8:11]\n"
    "v_add_f32 v230, v230, v232\n"
    "v_mov_b32 v2, v230\n"
    "v_mfma_f32_16x16x32_bf16 v[20:23], a[164:167], a[252:255], v[20:23]\n"
    "v_mul_f32 v235, s26, v235\n"
    "s_waitcnt lgkmcnt(1)\n"
    "v_lshlrev_b32 v236, 16, v58\n"
    "v_mfma_f32_16x16x32_bf16 v[68:71], a[168:171], a[252:255], v[68:71]\n"
    "v_permlane16_swap_b32 v230, v2\n"
    "v_add_f32 v230, v230, v2\n"
    "v_mfma_f32_16x16x32_bf16 v[76:79], a[172:175], a[252:255], v[76:79]\n"
    "v_mov_b32 v2, v230\n"
    "v_and_b32 v237, 0xffff0000, v58\n"
    "v_lshlrev_b32 v238, 16, v59\n"
    "v_permlane32_swap_b32 v230, v2\n"
    "s_waitcnt vmcnt(6)\n"
    "v_mfma_f32_16x16x32_bf16 v[64:67], a[184:187], v[96:99], v[64:67]\n"
    "v_add_f32 v230, v230, v2\n"
    "v_mul_f32 v230, s26, v230\n"
    "v_mfma_f32_16x16x32_bf16 v[72:75], a[188:191], v[96:99], v[72:75]\n"
    "v_fma_f32 v230, -v235, v235, v230\n"
    "v_max_f32 v230, 0, v230\n"
    "s_waitcnt vmcnt(5)\n"
    "v_mfma_f32_16x16x32_bf16 v[8:11], a[176:179], v[100:103], v[8:11]\n"
    "v_cvt_pk_bf16_f32 v230, v230, v230\n"
    "v_lshlrev_b32 v230, 16, v230\n"
    "v_mfma_f32_16x16x32_bf16 v[20:23], a[180:183], v[100:103], v[20:23]\n"
    "v_add_f32 v230, s28, v230\n"
    "v_cvt_pk_bf16_f32 v230, v230, v230\n"
    "v_mfma_f32_16x16x32_bf16 v[68:71], a[184:187], v[100:103], v[68:71]\n"
    "v_lshlrev_b32 v230, 16, v230\n"
    "v_sqrt_f32 v230, v230\n"
    "v_mfma_f32_16x16x32_bf16 v[76:79], a[188:191], v[100:103], v[76:79]\n"
    "v_and_b32 v239, 0xffff0000, v59\n"
    "v_cvt_pk_bf16_f32 v230, v230, v230\n"
    "v_lshlrev_b32 v230, 16, v230\n"
    "v_rcp_f32 v6, v230\n"
    "s_waitcnt vmcnt(4)\n"
    "v_mfma_f32_16x16x32_bf16 v[64:67], a[200:203], v[104:107], v[64:67]\n"
    "s_waitcnt lgkmcnt(0)\n"
    "v_lshlrev_b32 v240, 16, v60\n"
    "v_mul_f32 v6, v6, v6\n"
    "v_mfma_f32_16x16x32_bf16 v[72:75], a[204:207], v[104:107], v[72:75]\n"
    "v_and_b32 v241, 0xffff0000, v60\n"
    "v_lshlrev_b32 v242, 16, v61\n"
    "s_waitcnt vmcnt(2)\n"
    "v_mfma_f32_16x16x32_bf16 v[8:11], a[192:195], v[108:111], v[8:11]\n"
    "v_and_b32 v243, 0xffff0000, v61\n"
    "v_mfma_f32_16x16x32_bf16 v[20:23], a[196:199], v[108:111], v[20:23]\n"
    "v_mfma_f32_16x16x32_bf16 v[68:71], a[200:203], v[108:111], v[68:71]\n"
    "v_mfma_f32_16x16x32_bf16 v[76:79], a[204:207], v[108:111], v[76:79]\n"
    "s_waitcnt vmcnt(1)\n"
    "v_mfma_f32_16x16x32_bf16 v[64:67], a[216:219], v[80:83], v[64:67]\n"
    "v_mfma_f32_16x16x32_bf16 v[72:75], a[220:223], v[80:83], v[72:75]\n"
    "s_waitcnt vmcnt(0)\n"
    "v_mfma_f32_16x16x32_bf16 v[8:11], a[208:211], v[84:87], v[8:11]\n"
    "v_mfma_f32_16x16x32_bf16 v[20:23], a[212:215], v[84:87], v[20:23]\n"
    "v_mfma_f32_16x16x32_bf16 v[68:71], a[216:219], v[84:87], v[68:71]\n"
    "v_mfma_f32_16x16x32_bf16 v[76:79], a[220:223], v[84:87], v[76:79]\n"
    "v_mfma_f32_16x16x32_bf16 v[252:255], a[96:99], v[216:219], v[252:255]\n"
    "v_mfma_f32_16x16x32_bf16 v[16:19], a[100:103], v[216:219], v[16:19]\n"
    "v_mfma_f32_16x16x32_bf16 v[252:255], a[112:115], a[224:227], v[252:255]\n"
    "s_nop 1\n"
    "ds_write_b128 v246, v[8:11] offset:24576\n"
    "v_mfma_f32_16x16x32_bf16 v[16:19], a[116:119], a[224:227], v[16:19]\n"
    "v_mfma_f32_16x16x32_bf16 v[252:255], a[128:131], a[232:235], v[252:255]\n"
    "ds_write_b128 v246, v[20:23] offset:25600\n"
    "v_mfma_f32_16x16x32_bf16 v[16:19], a[132:135], a[232:235], v[16:19]\n"
    "v_mfma_f32_16x16x32_bf16 v[252:255], a[144:147], a[240:243], v[252:255]\n"
    "ds_write_b128 v247, v[64:67] offset:24576\n"
    "v_mfma_f32_16x16x32_bf16 v[16:19], a[148:151], a[240:243], v[16:19]\n"
    "v_mfma_f32_16x16x32_bf16 v[252:255], a[160:163], a[248:251], v[252:255]\n"
    "v_mfma_f32_16x16x32_bf16 v[16:19], a[164:167], a[248:251], v[16:19]\n"
    "ds_write_b128 v248, v[68:71] offset:24576\n"
    "v_mfma_f32_16x16x32_bf16 v[252:255], a[176:179], v[96:99], v[252:255]\n"
    "v_mfma_f32_16x16x32_bf16 v[16:19], a[180:183], v[96:99], v[16:19]\n"
    "ds_write_b128 v247, v[72:75] offset:25600\n"
    "v_mfma_f32_16x16x32_bf16 v[252:255], a[192:195], v[104:107], v[252:255]\n"
    "v_mfma_f32_16x16x32_bf16 v[16:19], a[196:199], v[104:107], v[16:19]\n"
    "ds_write_b128 v248, v[76:79] offset:25600\n"
    "v_mfma_f32_16x16x32_bf16 v[252:255], a[208:211], v[80:83], v[252:255]\n"
    "v_mfma_f32_16x16x32_bf16 v[16:19], a[212:215], v[80:83], v[16:19]\n"
    "s_waitcnt lgkmcnt(0)\n"
    "s_barrier\n"
    "ds_read_b128 v[144:147], v249 offset:24576\n"
    "ds_read_b128 v[148:151], v249 offset:25600\n"
    "ds_read_b128 v[152:155], v249 offset:26624\n"
    "ds_read_b128 v[156:159], v249 offset:27648\n"
    "ds_read_b128 v[160:163], v249 offset:28672\n"
    "ds_read_b128 v[164:167], v249 offset:29696\n"
    "s_waitcnt lgkmcnt(5)\n"
    "v_pk_add_f32 v[252:253], v[252:253], v[144:145]\n"
    "v_pk_add_f32 v[254:255], v[254:255], v[146:147]\n"
    "s_waitcnt lgkmcnt(4)\n"
    "v_pk_add_f32 v[16:17], v[16:17], v[148:149]\n"
    "v_pk_add_f32 v[18:19], v[18:19], v[150:151]\n"
    "s_waitcnt lgkmcnt(3)\n"
    "v_pk_add_f32 v[252:253], v[252:253], v[152:153]\n"
    "v_pk_add_f32 v[254:255], v[254:255], v[154:155]\n"
    "s_waitcnt lgkmcnt(2)\n"
    "v_pk_add_f32 v[16:17], v[16:17], v[156:157]\n"
    "v_pk_add_f32 v[18:19], v[18:19], v[158:159]\n"
    "s_waitcnt lgkmcnt(1)\n"
    "v_pk_add_f32 v[252:253], v[252:253], v[160:161]\n"
    "v_pk_add_f32 v[254:255], v[254:255], v[162:163]\n"
    "s_waitcnt lgkmcnt(0)\n"
    "v_pk_add_f32 v[16:17], v[16:17], v[164:165]\n"
    "v_pk_add_f32 v[18:19], v[18:19], v[166:167]\n"
    "v_pk_fma_f32 v[252:253], v[252:253], v[6:7], v[236:237] op_sel_hi:[1,0,1]\n"
    "v_cvt_pk_bf16_f32 v26, v252, v253\n"
    "v_pk_fma_f32 v[254:255], v[254:255], v[6:7], v[238:239] op_sel_hi:[1,0,1]\n"
    "v_cvt_pk_bf16_f32 v27, v254, v255\n"
    "v_pk_fma_f32 v[16:17], v[16:17], v[6:7], v[240:241] op_sel_hi:[1,0,1]\n"
    "v_cvt_pk_bf16_f32 v28, v16, v17\n"
    "v_pk_fma_f32 v[18:19], v[18:19], v[6:7], v[242:243] op_sel_hi:[1,0,1]\n"
    "v_cvt_pk_bf16_f32 v29, v18, v19\n"
    "v_permlane16_swap_b32 v26, v28\n"
    "s_nop 0\n"
    "v_permlane16_swap_b32 v27, v29\n"
    "s_mov_b32 s67, 1048576\n"
    "buffer_store_dwordx4 v[26:29], v245, s[52:55], s67 offen\n"
    "s_cmp_eq_u32 s3, 0\n"
    "s_cselect_b32 s38, 64, 0\n"
    "s_mov_b64 exec, 1\n"
    "buffer_store_dword v62, v50, s[36:39], 0 offen sc1\n"
    "s_mov_b64 exec, -1\n"
    "s_endpgm\n"
    ".Lmk_end:\n"
    : "+v"(tid)
    : "s"(__builtin_amdgcn_kernarg_segment_ptr()), "s"(blockIdx.x)
    : "v1", "v2", "v3", "v4", "v5", "v6", "v7", "v8", "v9", "v10", "v11", "v12", "v13", "v14", "v15", "v16", "v17", "v18", "v19", "v20", "v21", "v22", "v23", "v24", "v25", "v26", "v27", "v28", "v29", "v30", "v31", "v32", "v33", "v34", "v35", "v36", "v37", "v38", "v39", "v40", "v41", "v42", "v43", "v44", "v45", "v46", "v47", "v48", "v49", "v50", "v51", "v52", "v53", "v54", "v55", "v56", "v57", "v58", "v59", "v60", "v61", "v62", "v63", "v64", "v65", "v66", "v67", "v68", "v69", "v70", "v71", "v72", "v73", "v74", "v75", "v76", "v77", "v78", "v79", "v80", "v81", "v82", "v83", "v84", "v85", "v86", "v87", "v88", "v89", "v90", "v91", "v92", "v93", "v94", "v95", "v96", "v97", "v98", "v99", "v100", "v101", "v102", "v103", "v104", "v105", "v106", "v107", "v108", "v109", "v110", "v111", "v112", "v113", "v114", "v115", "v116", "v117", "v118", "v119", "v120", "v121", "v122", "v123", "v124", "v125", "v126", "v127", "v128", "v129", "v130", "v131", "v132", "v133", "v134", "v135", "v136", "v137", "v138", "v139", "v140", "v141", "v142", "v143", "v144", "v145", "v146", "v147", "v148", "v149", "v150", "v151", "v152", "v153", "v154", "v155", "v156", "v157", "v158", "v159", "v160", "v161", "v162", "v163", "v164", "v165", "v166", "v167", "v168", "v169", "v170", "v171", "v172", "v173", "v174", "v175", "v176", "v177", "v178", "v179", "v180", "v181", "v182", "v183", "v184", "v185", "v186", "v187", "v188", "v189", "v190", "v191", "v192", "v193", "v194", "v195", "v196", "v197", "v198", "v199", "v200", "v201", "v202", "v203", "v204", "v205", "v206", "v207", "v208", "v209", "v210", "v211", "v212", "v213", "v214", "v215", "v216", "v217", "v218", "v219", "v220", "v221", "v222", "v223", "v224", "v225", "v226", "v227", "v228", "v229", "v230", "v231", "v232", "v233", "v234", "v235", "v236", "v237", "v238", "v239", "v240", "v241", "v242", "v243", "v244", "v245", "v246", "v247", "v248", "v249", "v250", "v251", "v252", "v253", "v254", "v255", "a0", "a1", "a2", "a3", "a4", "a5", "a6", "a7", "a8", "a9", "a10", "a11", "a12", "a13", "a14", "a15", "a16", "a17", "a18", "a19", "a20", "a21", "a22", "a23", "a24", "a25", "a26", "a27", "a28", "a29", "a30", "a31", "a32", "a33", "a34", "a35", "a36", "a37", "a38", "a39", "a40", "a41", "a42", "a43", "a44", "a45", "a46", "a47", "a48", "a49", "a50", "a51", "a52", "a53", "a54", "a55", "a56", "a57", "a58", "a59", "a60", "a61", "a62", "a63", "a64", "a65", "a66", "a67", "a68", "a69", "a70", "a71", "a72", "a73", "a74", "a75", "a76", "a77", "a78", "a79", "a80", "a81", "a82", "a83", "a84", "a85", "a86", "a87", "a88", "a89", "a90", "a91", "a92", "a93", "a94", "a95", "a96", "a97", "a98", "a99", "a100", "a101", "a102", "a103", "a104", "a105", "a106", "a107", "a108", "a109", "a110", "a111", "a112", "a113", "a114", "a115", "a116", "a117", "a118", "a119", "a120", "a121", "a122", "a123", "a124", "a125", "a126", "a127", "a128", "a129", "a130", "a131", "a132", "a133", "a134", "a135", "a136", "a137", "a138", "a139", "a140", "a141", "a142", "a143", "a144", "a145", "a146", "a147", "a148", "a149", "a150", "a151", "a152", "a153", "a154", "a155", "a156", "a157", "a158", "a159", "a160", "a161", "a162", "a163", "a164", "a165", "a166", "a167", "a168", "a169", "a170", "a171", "a172", "a173", "a174", "a175", "a176", "a177", "a178", "a179", "a180", "a181", "a182", "a183", "a184", "a185", "a186", "a187", "a188", "a189", "a190", "a191", "a192", "a193", "a194", "a195", "a196", "a197", "a198", "a199", "a200", "a201", "a202", "a203", "a204", "a205", "a206", "a207", "a208", "a209", "a210", "a211", "a212", "a213", "a214", "a215", "a216", "a217", "a218", "a219", "a220", "a221", "a222", "a223", "a224", "a225", "a226", "a227", "a228", "a229", "a230", "a231", "a232", "a233", "a234", "a235", "a236", "a237", "a238", "a239", "a240", "a241", "a242", "a243", "a244", "a245", "a246", "a247", "a248", "a249", "a250", "a251", "a252", "a253", "a254", "a255", "s3", "s4", "s5", "s6", "s7", "s8", "s9", "s10", "s11", "s12", "s13", "s14", "s15", "s16", "s17", "s18", "s19", "s20", "s21", "s22", "s23", "s24", "s25", "s26", "s27", "s28", "s29", "s30", "s31", "s33", "s34", "s35", "s36", "s37", "s38", "s39", "s40", "s41", "s42", "s43", "s44", "s45", "s46", "s47", "s48", "s49", "s50", "s51", "s52", "s53", "s54", "s55", "s56", "s57", "s58", "s59", "s60", "s61", "s62", "s63", "s64", "s65", "s66", "s67", "s68", "s69", "s70", "s71", "s72", "s73", "s74", "s75", "s76", "s77", "s78", "s79", "s80", "s81", "s82", "s83", "s84", "s85", "s86", "s87", "s88", "s89", "s90", "s91", "s92", "s93", "s94", "s95", "s96", "s97", "s98", "s99", "vcc", "scc", "memory");
}

extern "C" void kernel_launch(void* const* d_in, const int* in_sizes, int n_in, void* d_out, int out_size, void* d_ws,
                              size_t ws_size, hipStream_t stream) {
  s4_glu_block<<<256, 256, 0, stream>>>(d_in[0], d_in[1], d_in[2], d_in[3], d_in[4], d_in[5], d_in[6], d_in[7], d_in[8],
                                        d_in[10], d_in[11], d_in[12], d_out, d_ws);
}
```
